```python
import torch

def _softmax(x, axis=-1):
    return torch.softmax(x, dim=axis)

def _gelu(x):
    return torch.nn.functional.gelu(x, approximate='tanh')

def _silu(x):
    return torch.nn.functional.silu(x)

def _sigmoid(x):
    return torch.sigmoid(x)

def _layernorm(x, axis=-1, eps=1e-05):
    m = x.mean(dim=axis, keepdim=True)
    v = x.var(dim=axis, keepdim=True, unbiased=False)
    return (x - m) / torch.sqrt(v + eps)

def _scatter_add(tgt, idx, src, axis=0):
    return tgt.clone().index_add_(axis, idx, src)

def _segment_sum(src, seg, n):
    out = torch.zeros((n,) + src.shape[1:], dtype=src.dtype)
    return out.index_add_(0, seg, src)

def _pad(x, pw):
    flat = tuple((v for pair in reversed(pw) for v in pair))
    return torch.nn.functional.pad(x, flat)
kernel_name = 'parallel_sum__memory_aug_recurrent_mem_tr__dense_activation_gelu__prompt'

def setup_inputs():

    _w = lambda a: (lambda t: t.to(torch.bfloat16) if t.is_floating_point() else t)(torch.as_tensor(a))

    def _A_setup():
        B, S, Dm = (1, 2048, 1024)
        H, Dh = (32, 128)
        M, seg = (8, 32)
        seg = min(seg, S)
        S = S // seg * seg
        return {'x': _w((lambda *s: torch.randn(*s, dtype=torch.bfloat16) * 0.05)(B, S, Dm)), 'mem0': _w((lambda *s: torch.randn(*s, dtype=torch.bfloat16) * 0.2)(B, M, Dm)), 'Wq': _w((lambda *s: torch.randn(*s, dtype=torch.bfloat16) * 1.6)(Dm, H, Dh) * Dm ** (-0.5)), 'Wk': _w((lambda *s: torch.randn(*s, dtype=torch.bfloat16) * 1.6)(Dm, H, Dh) * Dm ** (-0.5)), 'Wv': _w((lambda *s: torch.randn(*s, dtype=torch.bfloat16) * 0.2)(Dm, H, Dh) * Dm ** (-0.5)), 'Wo': _w((lambda *s: torch.randn(*s, dtype=torch.bfloat16) * 0.2)(H, Dh, Dm) * (H * Dh) ** (-0.5)), 'seg_len': seg}
    _A_in = _A_setup()

    def _B_setup():
        B, S, D, F = (1, 2048, 1024, 3584)
        return {'x': _w((lambda *s: torch.randn(*s, dtype=torch.bfloat16) * 0.05)(B, S, D)), 'w1': _w((lambda *s: torch.randn(*s, dtype=torch.bfloat16) * 0.4)(D, F) * D ** (-0.5)), 'b1': _w((lambda *s: torch.randn(*s, dtype=torch.bfloat16) * 12.8)(F) * 0.01), 'w2': _w((lambda *s: torch.randn(*s, dtype=torch.bfloat16) * 0.1)(F, D) * F ** (-0.5)), 'b2': _w((lambda *s: torch.randn(*s, dtype=torch.bfloat16) * 1.6)(D) * 0.01)}
    _B_in = _B_setup()
    return {'A_x': _A_in['x'], 'A_mem0': _A_in['mem0'], 'A_Wq': _A_in['Wq'], 'A_Wk': _A_in['Wk'], 'A_Wv': _A_in['Wv'], 'A_Wo': _A_in['Wo'], 'A_seg_len': _A_in['seg_len'], 'B_x': _B_in['x'], 'B_w1': _B_in['w1'], 'B_b1': _B_in['b1'], 'B_w2': _B_in['w2'], 'B_b2': _B_in['b2']}

def reference(A_x, A_mem0, A_Wq, A_Wk, A_Wv, A_Wo, A_seg_len, B_x, B_w1, B_b1, B_w2, B_b2):

    def _A_ref(x, mem0, Wq, Wk, Wv, Wo, seg_len):
        B, S, Dm = x.shape
        H, Dh = (Wq.shape[1], Wq.shape[2])
        M = mem0.shape[1]
        scale = Dh ** (-0.5)
        n_seg = S // seg_len
        L = M + seg_len
        idx = torch.arange(L)
        mask = (idx[:, None] < M) | (idx[:, None] >= idx[None, :])
        mem = mem0
        outs = []
        for s in range(n_seg):
            xs = x[:, s * seg_len:(s + 1) * seg_len, :].clone()
            h = torch.concatenate([mem, xs], axis=1)
            q = torch.einsum('btd,dhe->bhte', h, Wq)
            k = torch.einsum('btd,dhe->bhte', h, Wk)
            v = torch.einsum('btd,dhe->bhte', h, Wv)
            scores = torch.einsum('bhqe,bhke->bhqk', q, k) * scale
            scores = torch.where(mask[None, None], scores, -1000000000.0)
            p = _softmax(scores, axis=-1)
            o = torch.einsum('bhqk,bhke->bhqe', p, v)
            o = torch.einsum('bhte,hed->btd', o, Wo)
            mem = o[:, :M, :].clone()
            outs.append(o[:, M:, :])
        return torch.concatenate(outs, axis=1)

    def _B_ref(x, w1, b1, w2, b2):
        h = torch.einsum('bsd,df->bsf', x, w1) + b1
        h = _gelu(h)
        return torch.einsum('bsf,fd->bsd', h, w2) + b2
    x = A_x
    xn = _layernorm(x, axis=-1)
    return x + _A_ref(xn, A_mem0, A_Wq, A_Wk, A_Wv, A_Wo, A_seg_len) + _B_ref(xn, B_w1, B_b1, B_w2, B_b2)
```

```cpp
#include <hip/hip_runtime.h>
#include <hip/hip_bf16.h>

typedef unsigned short u16;
typedef unsigned long long u64;
using bf16x8 = __attribute__((ext_vector_type(8))) short;
using bf16x4 = __attribute__((ext_vector_type(4))) short;
using f32x4 = __attribute__((ext_vector_type(4))) float;
using u32x4 = __attribute__((ext_vector_type(4))) unsigned;
typedef __attribute__((address_space(3))) bf16x4 lds_b4;
typedef __attribute__((address_space(1))) unsigned long long gu64;
typedef __attribute__((address_space(1))) unsigned gu32;

constexpr int S_ = 2048, DM = 1024, NH = 32, DH = 128, MM = 8, SEG = 32, NSEG = 64, FF = 3584;
constexpr int NQKV = 12288;
constexpr float SCALE = 0.08838834764831845f;

constexpr size_t WS_PART = 0;
constexpr size_t WS_PART_BYTES = 2ull * 4 * 32 * 32 * 64 * 8;
constexpr size_t WS_TMO = WS_PART + WS_PART_BYTES;
constexpr size_t WS_ZERO_BYTES = WS_PART_BYTES + 256;
constexpr size_t WS_XN = 8ull << 20;
constexpr size_t WS_QKV = 16ull << 20;
constexpr size_t WS_H = 72ull << 20;
constexpr size_t WS_O = 96ull << 20;
constexpr size_t WS_MEM = 120ull << 20;
constexpr size_t WS_QKVM = 122ull << 20;
constexpr size_t WS_SLAB = 140ull << 20;

__device__ __forceinline__ float bf2f(u16 v) { return __uint_as_float(((unsigned)v) << 16); }
__device__ __forceinline__ u16 f2bf(float f) {
  __hip_bfloat16 h = __float2bfloat16(f);
  return *reinterpret_cast<u16*>(&h);
}
__device__ __forceinline__ float rbf(float f) { return bf2f(f2bf(f)); }
__device__ __forceinline__ unsigned pack2(float a, float b) {
  unsigned r = (unsigned)f2bf(a) | ((unsigned)f2bf(b) << 16);
  return r == 0xFFFFFFFFu ? 0x7FFF7FFFu : r;
}

__global__ __launch_bounds__(256) void ln_kernel(const u16* __restrict__ x, const u16* __restrict__ mem0,
                                                  u16* __restrict__ xn, u16* memx, u16* qkvm) {
  int wave = threadIdx.x >> 6, lane = threadIdx.x & 63;
  int row = blockIdx.x * 4 + wave;
  const u16* xr = x + (size_t)row * DM;
  float v[16];
  bf16x8 a = *reinterpret_cast<const bf16x8*>(xr + lane * 8);
  bf16x8 b = *reinterpret_cast<const bf16x8*>(xr + 512 + lane * 8);
  float s = 0.f;
  for (int i = 0; i < 8; i++) { v[i] = bf2f((u16)a[i]); v[8 + i] = bf2f((u16)b[i]); s += v[i] + v[8 + i]; }
  for (int o = 32; o; o >>= 1) s += __shfl_xor(s, o);
  float m = s * (1.f / DM);
  float q = 0.f;
  for (int i = 0; i < 16; i++) { float d = v[i] - m; q += d * d; }
  for (int o = 32; o; o >>= 1) q += __shfl_xor(q, o);
  float r = rsqrtf(q * (1.f / DM) + 1e-5f);
  bf16x8 oa, ob;
  for (int i = 0; i < 8; i++) { oa[i] = (short)f2bf((v[i] - m) * r); ob[i] = (short)f2bf((v[8 + i] - m) * r); }
  *reinterpret_cast<bf16x8*>(xn + (size_t)row * DM + lane * 8) = oa;
  *reinterpret_cast<bf16x8*>(xn + (size_t)row * DM + 512 + lane * 8) = ob;
  size_t tid = (size_t)blockIdx.x * 256 + threadIdx.x, nth = (size_t)gridDim.x * 256;
  u32x4 P = {0xFFFFFFFFu, 0xFFFFFFFFu, 0xFFFFFFFFu, 0xFFFFFFFFu};
  for (size_t i = tid; i < (size_t)NSEG * MM * DM / 8; i += nth) {
    u32x4 val = P;
    if (i < MM * DM / 8) val = reinterpret_cast<const u32x4*>(mem0)[i];
    reinterpret_cast<u32x4*>(memx)[i] = val;
  }
  for (size_t i = tid; i < (size_t)NSEG * MM * NQKV / 8; i += nth) reinterpret_cast<u32x4*>(qkvm)[i] = P;
}

constexpr int BM = 256, BN = 128, BK = 64;
constexpr int A_BYTES = BM * BK * 2, B_BYTES = BK * BN * 2, STAGE_BYTES = A_BYTES + B_BYTES;

struct GemmP {
  const u16* xn; const u16* Wq; const u16* Wk; const u16* Wv; const u16* w1; const u16* b1;
  u16* qkv; u16* H;
  const u16* O; const u16* Wo; const u16* w2;
  float* slab;
};

__device__ __forceinline__ float gelu_tanh(float x) {
  float u = 0.7978845608028654f * (x + 0.044715f * x * x * x);
  float t = 1.f - 2.f / (__expf(2.f * u) + 1.f);
  return 0.5f * x * (1.f + t);
}

template <int MODE>
__global__ __launch_bounds__(512, 2) void gemm_kernel(GemmP p) {
  extern __shared__ __attribute__((aligned(16))) char smem[];
  const int tid = threadIdx.x, wid = tid >> 6, lane = tid & 63, fr = lane & 15, fq = lane >> 4;
  const int wr = wid >> 1, wc = wid & 1;
  int m0, n0, kt0, nkt;
  if (MODE == 0) {
    int b = blockIdx.x;
    m0 = (b & 7) * BM; n0 = (b >> 3) * BN; kt0 = 0; nkt = DM / BK;
  } else {
    int b = blockIdx.x;
    m0 = (b & 7) * BM; n0 = ((b >> 3) & 7) * BN; kt0 = (b >> 6) * 30; nkt = 30;
  }
  const u16* Bm = nullptr; int ldb = 0, bcol = n0;
  if (MODE == 0) {
    if (n0 < 4096) { Bm = p.Wq; ldb = 4096; }
    else if (n0 < 8192) { Bm = p.Wk; ldb = 4096; bcol = n0 - 4096; }
    else if (n0 < 12288) { Bm = p.Wv; ldb = 4096; bcol = n0 - 8192; }
    else { Bm = p.w1; ldb = FF; bcol = n0 - 12288; }
  }
  auto stage = [&](int buf, int kt) {
    const u16* Ab; int lda; const u16* Bb; int lb; int ka, kb;
    if (MODE == 0) { Ab = p.xn; lda = DM; Bb = Bm; lb = ldb; ka = kt * BK; kb = kt * BK; }
    else {
      if (kt < 64) { Ab = p.O; lda = 4096; Bb = p.Wo; lb = DM; ka = kt * BK; kb = ka; }
      else { Ab = p.H; lda = FF; Bb = p.w2; lb = DM; ka = (kt - 64) * BK; kb = ka; }
    }
    char* sa = smem + buf * STAGE_BYTES;
    char* sb = sa + A_BYTES;
#pragma unroll
    for (int i = 0; i < 4; i++) {
      int pb = (wid * 4 + i) * 1024 + lane * 16;
      int row = pb >> 7, chs = (pb >> 4) & 7, ch = chs ^ ((row >> 1) & 7);
      __builtin_amdgcn_global_load_lds((const unsigned*)(Ab + (size_t)(m0 + row) * lda + ka + ch * 8),
                                       (unsigned*)(sa + (wid * 4 + i) * 1024), 16, 0, 0);
    }
#pragma unroll
    for (int i = 0; i < 2; i++) {
      int pb = (wid * 2 + i) * 1024 + lane * 16;
      int row = pb >> 8, chs = (pb >> 4) & 15, ch = chs ^ (((row & 3) << 2) | ((row >> 2) & 3));
      __builtin_amdgcn_global_load_lds((const unsigned*)(Bb + (size_t)(kb + row) * lb + bcol + ch * 8),
                                       (unsigned*)(sb + (wid * 2 + i) * 1024), 16, 0, 0);
    }
  };
  f32x4 acc[4][4];
#pragma unroll
  for (int i = 0; i < 4; i++)
#pragma unroll
    for (int j = 0; j < 4; j++) acc[i][j] = f32x4{0.f, 0.f, 0.f, 0.f};
  stage(0, kt0);
  for (int t = 0; t < nkt; t++) {
    __syncthreads();
    if (t + 1 < nkt) stage((t + 1) & 1, kt0 + t + 1);
    char* sa = smem + (t & 1) * STAGE_BYTES;
    char* sb = sa + A_BYTES;
#pragma unroll
    for (int ks = 0; ks < 2; ks++) {
      bf16x8 af[4], bfr[4];
#pragma unroll
      for (int m = 0; m < 4; m++) {
        int row = wr * 64 + m * 16 + fr, ch = ks * 4 + fq;
        af[m] = *reinterpret_cast<const bf16x8*>(sa + row * 128 + 16 * (ch ^ ((row >> 1) & 7)));
      }
#pragma unroll
      for (int n = 0; n < 4; n++) {
        int c = wc * 4 + n;
        int q = fr >> 2, pp = fr & 3;
        bf16x4 lo, hi;
        {
          int row = ks * 32 + 8 * fq + q, ch = 2 * c + (pp >> 1);
          int off = 256 * row + 16 * (ch ^ (((row & 3) << 2) | ((row >> 2) & 3))) + 8 * (pp & 1);
          lo = __builtin_amdgcn_ds_read_tr16_b64_v4i16((lds_b4*)(sb + off));
        }
        {
          int row = ks * 32 + 8 * fq + 4 + q, ch = 2 * c + (pp >> 1);
          int off = 256 * row + 16 * (ch ^ (((row & 3) << 2) | ((row >> 2) & 3))) + 8 * (pp & 1);
          hi = __builtin_amdgcn_ds_read_tr16_b64_v4i16((lds_b4*)(sb + off));
        }
        bfr[n] = bf16x8{lo[0], lo[1], lo[2], lo[3], hi[0], hi[1], hi[2], hi[3]};
      }
#pragma unroll
      for (int m = 0; m < 4; m++)
#pragma unroll
        for (int n = 0; n < 4; n++)
          acc[m][n] = __builtin_amdgcn_mfma_f32_16x16x32_bf16(bfr[n], af[m], acc[m][n], 0, 0, 0);
    }
  }
#pragma unroll
  for (int m = 0; m < 4; m++) {
    int row = m0 + wr * 64 + m * 16 + fr;
#pragma unroll
    for (int n = 0; n < 4; n++) {
      int col = n0 + wc * 64 + n * 16 + fq * 4;
      if (MODE == 0) {
        bf16x4 o;
        if (n0 < NQKV) {
          for (int r = 0; r < 4; r++) o[r] = (short)f2bf(acc[m][n][r]);
          *reinterpret_cast<bf16x4*>(p.qkv + (size_t)row * NQKV + col) = o;
        } else {
          int hc = col - NQKV;
          for (int r = 0; r < 4; r++) o[r] = (short)f2bf(gelu_tanh(rbf(rbf(acc[m][n][r]) + bf2f(p.b1[hc + r]))));
          *reinterpret_cast<bf16x4*>(p.H + (size_t)row * FF + hc) = o;
        }
      } else {
        int ks = blockIdx.x >> 6;
        *reinterpret_cast<f32x4*>(p.slab + ((size_t)ks * S_ + row) * DM + col) = acc[m][n];
      }
    }
  }
}

constexpr int KROW = 272;
constexpr int R_RED = 0;
constexpr int R_K = 24576;
constexpr int R_V = R_K + 48 * KROW;
constexpr int R_Q = R_V + 64 * KROW;
constexpr int R_RED2 = R_Q + 8 * KROW;
constexpr int R_STG = R_RED2 + 2048;
constexpr int R_LDS = R_STG + 65536;

__device__ __forceinline__ u32x4 ld_sc1(const void* ptr) {
  u32x4 v;
  asm volatile("global_load_dwordx4 %0, %1, off sc1\n\ts_waitcnt vmcnt(0)" : "=&v"(v) : "v"(ptr) : "memory");
  return v;
}

__global__ __launch_bounds__(512, 2) void rec_kernel(const u16* __restrict__ Wq, const u16* __restrict__ Wk,
                                                     const u16* __restrict__ Wv, const u16* __restrict__ Wo,
                                                     const u16* __restrict__ qkvt, u16* memx, u16* qkvm,
                                                     u64* part, unsigned* tmo) {
  extern __shared__ __attribute__((aligned(16))) char smem[];
  const int tid = threadIdx.x, w = tid >> 6, lane = tid & 63, fr = lane & 15, g = lane >> 4;
  const int h = blockIdx.x >> 2, j = blockIdx.x & 3;
  bf16x8 wb[6][4];
  bf16x8 wo[2][4];
  {
    char* stg = smem + R_STG + w * 8192;
    for (int mat = 0; mat < 3; mat++) {
      const u16* W = mat == 0 ? Wq : (mat == 1 ? Wk : Wv);
      for (int i = 0; i < 8; i++) {
        int c = i * 64 + lane, row = c >> 2, ch = c & 3;
        u32x4 v = *reinterpret_cast<const u32x4*>(W + (size_t)(128 * w + row) * 4096 + h * 128 + 32 * j + ch * 8);
        *reinterpret_cast<u32x4*>(stg + row * 64 + ch * 16) = v;
      }
      __builtin_amdgcn_s_waitcnt(0);
      __builtin_amdgcn_wave_barrier();
      for (int t2 = 0; t2 < 2; t2++)
        for (int ks = 0; ks < 4; ks++) {
          int q = fr >> 2, pp = fr & 3;
          bf16x4 lo = __builtin_amdgcn_ds_read_tr16_b64_v4i16((lds_b4*)(stg + (ks * 32 + 8 * g + q) * 64 + t2 * 32 + pp * 8));
          bf16x4 hi = __builtin_amdgcn_ds_read_tr16_b64_v4i16((lds_b4*)(stg + (ks * 32 + 8 * g + 4 + q) * 64 + t2 * 32 + pp * 8));
          wb[mat * 2 + t2][ks] = bf16x8{lo[0], lo[1], lo[2], lo[3], hi[0], hi[1], hi[2], hi[3]};
        }
      __builtin_amdgcn_s_waitcnt(0);
      __builtin_amdgcn_wave_barrier();
    }
    for (int i = 0; i < 8; i++) {
      int c = i * 64 + lane, row = c >> 2, ch = c & 3;
      u32x4 v = *reinterpret_cast<const u32x4*>(Wo + (size_t)(h * 128 + row) * DM + 256 * j + 32 * w + ch * 8);
      *reinterpret_cast<u32x4*>(stg + row * 64 + ch * 16) = v;
    }
    __builtin_amdgcn_s_waitcnt(0);
    __builtin_amdgcn_wave_barrier();
    for (int t2 = 0; t2 < 2; t2++)
      for (int kk = 0; kk < 4; kk++) {
        int q = fr >> 2, pp = fr & 3;
        bf16x4 lo = __builtin_amdgcn_ds_read_tr16_b64_v4i16((lds_b4*)(stg + (kk * 32 + 4 * g + q) * 64 + t2 * 32 + pp * 8));
        bf16x4 hi = __builtin_amdgcn_ds_read_tr16_b64_v4i16((lds_b4*)(stg + (kk * 32 + 16 + 4 * g + q) * 64 + t2 * 32 + pp * 8));
        wo[t2][kk] = bf16x8{lo[0], lo[1], lo[2], lo[3], hi[0], hi[1], hi[2], hi[3]};
      }
  }
  for (int i = tid; i < 8 * 17; i += 512) *reinterpret_cast<u32x4*>(smem + R_K + 40 * KROW + i * 16) = u32x4{0, 0, 0, 0};
  for (int i = tid; i < 24 * 17; i += 512) *reinterpret_cast<u32x4*>(smem + R_V + 40 * KROW + i * 16) = u32x4{0, 0, 0, 0};
  __syncthreads();

  unsigned spin_cap = 4000000;
  for (int s = 0; s < NSEG; s++) {
    {
      int c = tid & 255, which = tid >> 8;
      int row = c >> 4, ch = c & 15;
      for (int half = 0; half < 2; half++) {
        int r2 = row + 16 * half;
        u32x4 v = *reinterpret_cast<const u32x4*>(qkvt + (size_t)(s * SEG + r2) * NQKV + 4096 * (1 + which) + h * 128 + ch * 8);
        *reinterpret_cast<u32x4*>(smem + (which ? R_V : R_K) + (8 + r2) * KROW + ch * 16) = v;
      }
    }
    bf16x8 af[4];
    {
      const u16* mp = memx + (size_t)s * MM * DM + (size_t)(fr & 7) * DM + 128 * w + 8 * g;
      unsigned spins = 0;
      while (true) {
        bool ok = true;
        for (int ks = 0; ks < 4; ks++) {
          u32x4 v = ld_sc1(mp + 32 * ks);
          ok &= (v[0] != 0xFFFFFFFFu) & (v[1] != 0xFFFFFFFFu) & (v[2] != 0xFFFFFFFFu) & (v[3] != 0xFFFFFFFFu);
          af[ks] = __builtin_bit_cast(bf16x8, v);
        }
        if (__all(ok)) break;
        if (++spins > spin_cap) { if (lane == 0) __hip_atomic_store((gu32*)tmo, 1u, __ATOMIC_RELAXED, __HIP_MEMORY_SCOPE_AGENT); break; }
      }
    }
    {
      f32x4 acc[6];
      for (int t = 0; t < 6; t++) acc[t] = f32x4{0.f, 0.f, 0.f, 0.f};
      for (int ks = 0; ks < 4; ks++)
        for (int t = 0; t < 6; t++) acc[t] = __builtin_amdgcn_mfma_f32_16x16x32_bf16(af[ks], wb[t][ks], acc[t], 0, 0, 0);
      if (g < 2) {
        float* red = reinterpret_cast<float*>(smem + R_RED) + w * 768;
        for (int t = 0; t < 6; t++)
          for (int r = 0; r < 4; r++) red[t * 128 + (4 * g + r) * 16 + fr] = acc[t][r];
      }
    }
    __syncthreads();
    if (tid < 384) {
      int t = tid >> 6, row = (tid >> 3) & 7, cp = tid & 7;
      float a0 = 0.f, a1 = 0.f;
      const float* red = reinterpret_cast<const float*>(smem + R_RED) + t * 128 + row * 16 + cp * 2;
      for (int ww = 0; ww < 8; ww++) { a0 += red[ww * 768]; a1 += red[ww * 768 + 1]; }
      unsigned pk = pack2(a0, a1);
      int mat = t >> 1;
      u16* dst = qkvm + ((size_t)s * MM + row) * NQKV + mat * 4096 + h * 128 + 32 * j + (t & 1) * 16 + cp * 2;
      __hip_atomic_store((gu32*)dst, pk, __ATOMIC_RELAXED, __HIP_MEMORY_SCOPE_AGENT);
    }
    if (tid < 384) {
      int mat = tid >> 7, row = (tid >> 4) & 7, ch = tid & 15;
      const u16* src = qkvm + ((size_t)s * MM + row) * NQKV + mat * 4096 + h * 128 + ch * 8;
      unsigned spins = 0;
      u32x4 v;
      while (true) {
        v = ld_sc1(src);
        bool ok = (v[0] != 0xFFFFFFFFu) & (v[1] != 0xFFFFFFFFu) & (v[2] != 0xFFFFFFFFu) & (v[3] != 0xFFFFFFFFu);
        if (__all(ok)) break;
        if (++spins > spin_cap) { if (lane == 0) __hip_atomic_store((gu32*)tmo, 2u, __ATOMIC_RELAXED, __HIP_MEMORY_SCOPE_AGENT); break; }
      }
      char* dst = smem + (mat == 0 ? R_Q : (mat == 1 ? R_K : R_V)) + row * KROW + ch * 16;
      *reinterpret_cast<u32x4*>(dst) = v;
    }
    __syncthreads();
    f32x4 st[3];
    for (int t = 0; t < 3; t++) st[t] = f32x4{0.f, 0.f, 0.f, 0.f};
    for (int ks = 0; ks < 4; ks++) {
      bf16x8 qf = *reinterpret_cast<const bf16x8*>(smem + R_Q + (fr & 7) * KROW + ks * 64 + g * 16);
      for (int t = 0; t < 3; t++) {
        bf16x8 kf = *reinterpret_cast<const bf16x8*>(smem + R_K + (16 * t + fr) * KROW + ks * 64 + g * 16);
        st[t] = __builtin_amdgcn_mfma_f32_16x16x32_bf16(kf, qf, st[t], 0, 0, 0);
      }
    }
    float mx = -3.0e38f;
    for (int t = 0; t < 3; t++)
      for (int r = 0; r < 4; r++) {
        float v = rbf(rbf(st[t][r]) * SCALE);
        if (t == 2 && g >= 2) v = -3.0e38f;
        st[t][r] = v;
        mx = fmaxf(mx, v);
      }
    mx = fmaxf(mx, __shfl_xor(mx, 16));
    mx = fmaxf(mx, __shfl_xor(mx, 32));
    float sum = 0.f;
    for (int t = 0; t < 3; t++)
      for (int r = 0; r < 4; r++) {
        float e = (t == 2 && g >= 2) ? 0.f : __expf(st[t][r] - mx);
        st[t][r] = e;
        sum += e;
      }
    sum += __shfl_xor(sum, 16);
    sum += __shfl_xor(sum, 32);
    float inv = 1.f / sum;
    bf16x8 pf[2];
    for (int r = 0; r < 4; r++) {
      pf[0][r] = (short)f2bf(st[0][r] * inv);
      pf[0][4 + r] = (short)f2bf(st[1][r] * inv);
      pf[1][r] = (short)f2bf(st[2][r] * inv);
      pf[1][4 + r] = 0;
    }
    bf16x8 of[4];
    {
      int q = fr >> 2, pp = fr & 3;
      for (int et = 0; et < 8; et++) {
        f32x4 o = {0.f, 0.f, 0.f, 0.f};
        for (int ks = 0; ks < 2; ks++) {
          bf16x4 lo = __builtin_amdgcn_ds_read_tr16_b64_v4i16((lds_b4*)(smem + R_V + (32 * ks + 4 * g + q) * KROW + et * 32 + pp * 8));
          bf16x4 hi = __builtin_amdgcn_ds_read_tr16_b64_v4i16((lds_b4*)(smem + R_V + (32 * ks + 16 + 4 * g + q) * KROW + et * 32 + pp * 8));
          bf16x8 vf = bf16x8{lo[0], lo[1], lo[2], lo[3], hi[0], hi[1], hi[2], hi[3]};
          o = __builtin_amdgcn_mfma_f32_16x16x32_bf16(vf, pf[ks], o, 0, 0, 0);
        }
        for (int r = 0; r < 4; r++) of[et >> 1][(et & 1) * 4 + r] = (short)f2bf(o[r]);
      }
    }
    {
      f32x4 pa[2];
      pa[0] = f32x4{0.f, 0.f, 0.f, 0.f}; pa[1] = pa[0];
      for (int kk = 0; kk < 4; kk++)
        for (int t2 = 0; t2 < 2; t2++) pa[t2] = __builtin_amdgcn_mfma_f32_16x16x32_bf16(of[kk], wo[t2][kk], pa[t2], 0, 0, 0);
      if (g < 2) {
        for (int t2 = 0; t2 < 2; t2++) {
          int col = 32 * w + 16 * t2 + fr;
          int dst = col >> 3, cc = col & 7;
          u64* base = part + ((((size_t)(s & 1) * 4 + j) * 32 + dst) * 32 + h) * 64;
          for (int r = 0; r < 4; r++) {
            u64 gr = ((u64)(unsigned)(s + 1) << 32) | (u64)__float_as_uint(pa[t2][r]);
            __hip_atomic_store((gu64*)(base + (4 * g + r) * 8 + cc), gr, __ATOMIC_RELAXED, __HIP_MEMORY_SCOPE_AGENT);
          }
        }
      }
    }
    if (s + 1 < NSEG) {
      int src = tid >> 4, idx = tid & 15;
      const u64* base = part + ((((size_t)(s & 1) * 4 + j) * 32 + h) * 32 + src) * 64 + idx * 4;
      float val[4];
      unsigned spins = 0;
      while (true) {
        u32x4 a = ld_sc1(base), b = ld_sc1(base + 2);
        bool ok = (a[1] == (unsigned)(s + 1)) & (a[3] == (unsigned)(s + 1)) & (b[1] == (unsigned)(s + 1)) & (b[3] == (unsigned)(s + 1));
        val[0] = __uint_as_float(a[0]); val[1] = __uint_as_float(a[2]); val[2] = __uint_as_float(b[0]); val[3] = __uint_as_float(b[2]);
        if (__all(ok)) break;
        if (++spins > spin_cap) { if (lane == 0) __hip_atomic_store((gu32*)tmo, 3u, __ATOMIC_RELAXED, __HIP_MEMORY_SCOPE_AGENT); break; }
      }
      for (int i = 0; i < 4; i++) { val[i] += __shfl_xor(val[i], 16); val[i] += __shfl_xor(val[i], 32); }
      if (lane < 16) {
        float* r2 = reinterpret_cast<float*>(smem + R_RED2) + w * 64 + lane * 4;
        for (int i = 0; i < 4; i++) r2[i] = val[i];
      }
    }
    __syncthreads();
    if (s + 1 < NSEG && tid < 32) {
      const float* r2 = reinterpret_cast<const float*>(smem + R_RED2) + tid * 2;
      float a0 = 0.f, a1 = 0.f;
      for (int ww = 0; ww < 8; ww++) { a0 += r2[ww * 64]; a1 += r2[ww * 64 + 1]; }
      int row = tid >> 2, cc = (tid & 3) * 2;
      u16* dst = memx + ((size_t)(s + 1) * MM + row) * DM + 256 * j + 8 * h + cc;
      __hip_atomic_store((gu32*)dst, pack2(a0, a1), __ATOMIC_RELAXED, __HIP_MEMORY_SCOPE_AGENT);
    }
  }
}

__global__ __launch_bounds__(128) void tok_attn_kernel(const u16* __restrict__ qkvt, const u16* __restrict__ qkvm,
                                                        u16* __restrict__ O) {
  __shared__ __attribute__((aligned(16))) char sm[48 * KROW + 64 * KROW];
  char* sK = sm; char* sV = sm + 48 * KROW;
  const int s = blockIdx.x >> 5, h = blockIdx.x & 31;
  const int tid = threadIdx.x, w = tid >> 6, lane = tid & 63, fr = lane & 15, g = lane >> 4;
  for (int c = tid; c < 40 * 16 * 2; c += 128) {
    int which = c / 640, cc = c % 640, row = cc >> 4, ch = cc & 15;
    const u16* src = row < 8 ? qkvm + ((size_t)s * MM + row) * NQKV : qkvt + (size_t)(s * SEG + row - 8) * NQKV;
    u32x4 v = *reinterpret_cast<const u32x4*>(src + 4096 * (1 + which) + h * 128 + ch * 8);
    *reinterpret_cast<u32x4*>((which ? sV : sK) + row * KROW + ch * 16) = v;
  }
  for (int i = tid; i < 8 * 17; i += 128) *reinterpret_cast<u32x4*>(sK + 40 * KROW + i * 16) = u32x4{0, 0, 0, 0};
  for (int i = tid; i < 24 * 17; i += 128) *reinterpret_cast<u32x4*>(sV + 40 * KROW + i * 16) = u32x4{0, 0, 0, 0};
  __syncthreads();
  const int qi = w * 16 + fr;
  const u16* qp = qkvt + (size_t)(s * SEG + qi) * NQKV + h * 128;
  f32x4 st[3];
  for (int t = 0; t < 3; t++) st[t] = f32x4{0.f, 0.f, 0.f, 0.f};
  for (int ks = 0; ks < 4; ks++) {
    bf16x8 qf = *reinterpret_cast<const bf16x8*>(qp + ks * 32 + g * 8);
    for (int t = 0; t < 3; t++) {
      bf16x8 kf = *reinterpret_cast<const bf16x8*>(sK + (16 * t + fr) * KROW + ks * 64 + g * 16);
      st[t] = __builtin_amdgcn_mfma_f32_16x16x32_bf16(kf, qf, st[t], 0, 0, 0);
    }
  }
  float mx = -3.0e38f;
  for (int t = 0; t < 3; t++)
    for (int r = 0; r < 4; r++) {
      int key = 16 * t + 4 * g + r;
      float v = rbf(rbf(st[t][r]) * SCALE);
      if (key >= 40 || key > qi + 8) v = -3.0e38f;
      st[t][r] = v;
      mx = fmaxf(mx, v);
    }
  mx = fmaxf(mx, __shfl_xor(mx, 16));
  mx = fmaxf(mx, __shfl_xor(mx, 32));
  float sum = 0.f;
  for (int t = 0; t < 3; t++)
    for (int r = 0; r < 4; r++) {
      int key = 16 * t + 4 * g + r;
      float e = (key >= 40 || key > qi + 8) ? 0.f : __expf(st[t][r] - mx);
      st[t][r] = e;
      sum += e;
    }
  sum += __shfl_xor(sum, 16);
  sum += __shfl_xor(sum, 32);
  float inv = 1.f / sum;
  bf16x8 pf[2];
  for (int r = 0; r < 4; r++) {
    pf[0][r] = (short)f2bf(st[0][r] * inv);
    pf[0][4 + r] = (short)f2bf(st[1][r] * inv);
    pf[1][r] = (short)f2bf(st[2][r] * inv);
    pf[1][4 + r] = 0;
  }
  int q = fr >> 2, pp = fr & 3;
  u16* op = O + (size_t)(s * SEG + qi) * 4096 + h * 128;
  for (int et = 0; et < 8; et++) {
    f32x4 o = {0.f, 0.f, 0.f, 0.f};
    for (int ks = 0; ks < 2; ks++) {
      bf16x4 lo = __builtin_amdgcn_ds_read_tr16_b64_v4i16((lds_b4*)(sV + (32 * ks + 4 * g + q) * KROW + et * 32 + pp * 8));
      bf16x4 hi = __builtin_amdgcn_ds_read_tr16_b64_v4i16((lds_b4*)(sV + (32 * ks + 16 + 4 * g + q) * KROW + et * 32 + pp * 8));
      bf16x8 vf = bf16x8{lo[0], lo[1], lo[2], lo[3], hi[0], hi[1], hi[2], hi[3]};
      o = __builtin_amdgcn_mfma_f32_16x16x32_bf16(vf, pf[ks], o, 0, 0, 0);
    }
    bf16x4 ov;
    for (int r = 0; r < 4; r++) ov[r] = (short)f2bf(o[r]);
    *reinterpret_cast<bf16x4*>(op + et * 16 + g * 4) = ov;
  }
}

__global__ __launch_bounds__(256) void final_kernel(const u16* __restrict__ x, const u16* __restrict__ b2,
                                                     const float* __restrict__ slab, u16* __restrict__ out) {
  size_t i = ((size_t)blockIdx.x * 256 + threadIdx.x) * 4;
  f32x4 a = *reinterpret_cast<const f32x4*>(slab + i);
  for (int k = 1; k < 4; k++) a += *reinterpret_cast<const f32x4*>(slab + (size_t)k * S_ * DM + i);
  bf16x4 xv = *reinterpret_cast<const bf16x4*>(x + i);
  bf16x4 bv = *reinterpret_cast<const bf16x4*>(b2 + (i & (DM - 1)));
  bf16x4 o;
  for (int r = 0; r < 4; r++) o[r] = (short)f2bf(bf2f((u16)xv[r]) + a[r] + bf2f((u16)bv[r]));
  *reinterpret_cast<bf16x4*>(out + i) = o;
}

extern "C" void kernel_launch(void* const* d_in, const int* in_sizes, int n_in, void* d_out, int out_size,
                              void* d_ws, size_t ws_size, hipStream_t stream) {
  const u16* x = (const u16*)d_in[0];
  const u16* mem0 = (const u16*)d_in[1];
  const u16* Wq = (const u16*)d_in[2];
  const u16* Wk = (const u16*)d_in[3];
  const u16* Wv = (const u16*)d_in[4];
  const u16* Wo = (const u16*)d_in[5];
  const u16* w1 = (const u16*)d_in[8];
  const u16* b1 = (const u16*)d_in[9];
  const u16* w2 = (const u16*)d_in[10];
  const u16* b2 = (const u16*)d_in[11];
  char* ws = (char*)d_ws;
  u16* xn = (u16*)(ws + WS_XN);
  u16* qkv = (u16*)(ws + WS_QKV);
  u16* H = (u16*)(ws + WS_H);
  u16* O = (u16*)(ws + WS_O);
  u16* memx = (u16*)(ws + WS_MEM);
  u16* qkvm = (u16*)(ws + WS_QKVM);
  float* slab = (float*)(ws + WS_SLAB);
  u64* part = (u64*)(ws + WS_PART);
  unsigned* tmo = (unsigned*)(ws + WS_TMO);

  hipFuncSetAttribute((const void*)gemm_kernel<0>, hipFuncAttributeMaxDynamicSharedMemorySize, 2 * STAGE_BYTES);
  hipFuncSetAttribute((const void*)gemm_kernel<1>, hipFuncAttributeMaxDynamicSharedMemorySize, 2 * STAGE_BYTES);
  hipFuncSetAttribute((const void*)rec_kernel, hipFuncAttributeMaxDynamicSharedMemorySize, R_LDS);

  hipMemsetAsync(ws + WS_PART, 0, WS_ZERO_BYTES, stream);
  ln_kernel<<<S_ / 4, 256, 0, stream>>>(x, mem0, xn, memx, qkvm);
  GemmP p;
  p.xn = xn; p.Wq = Wq; p.Wk = Wk; p.Wv = Wv; p.w1 = w1; p.b1 = b1; p.qkv = qkv; p.H = H;
  p.O = O; p.Wo = Wo; p.w2 = w2; p.slab = slab;
  gemm_kernel<0><<<8 * 124, 512, 2 * STAGE_BYTES, stream>>>(p);
  rec_kernel<<<128, 512, R_LDS, stream>>>(Wq, Wk, Wv, Wo, qkv, memx, qkvm, part, tmo);
  tok_attn_kernel<<<NSEG * NH, 128, 0, stream>>>(qkv, qkvm, O);
  gemm_kernel<1><<<256, 512, 2 * STAGE_BYTES, stream>>>(p);
  final_kernel<<<S_ * DM / 4 / 256, 256, 0, stream>>>(x, b2, slab, (u16*)d_out);
}
```

```cpp
#include <hip/hip_runtime.h>
#include <hip/hip_bf16.h>

typedef unsigned short u16;
typedef unsigned long long u64;
using bf16x8 = __attribute__((ext_vector_type(8))) short;
using bf16x4 = __attribute__((ext_vector_type(4))) short;
using f32x4 = __attribute__((ext_vector_type(4))) float;
using u32x4 = __attribute__((ext_vector_type(4))) unsigned;
typedef __attribute__((address_space(3))) bf16x4 lds_b4;
typedef __attribute__((address_space(1))) unsigned long long gu64;
typedef __attribute__((address_space(1))) unsigned gu32;

constexpr int S_ = 2048, DM = 1024, NH = 32, DH = 128, MM = 8, SEG = 32, NSEG = 64, FF = 3584;
constexpr int NQKV = 12288;
constexpr float SCALE = 0.08838834764831845f;

constexpr size_t WS_TMO = 0;
constexpr size_t WS_ZERO_BYTES = 8192;
constexpr int C_TMO = 0, C_TICKET = 32, C_Q = 64, C_H = 64 + 8 * 32, C_A = 64 + 16 * 32, C_F = 64 + 32 * 32, C_KV = 64 + 48 * 32;
constexpr size_t WS_PART = 1ull << 20;
constexpr size_t WS_PART_BYTES = 4ull * 4 * 32 * 32 * 64 * 4;
constexpr size_t WS_XN = 8ull << 20;
constexpr size_t WS_QKV = 16ull << 20;
constexpr size_t WS_H = 72ull << 20;
constexpr size_t WS_O = 96ull << 20;
constexpr size_t WS_MEM = 120ull << 20;
constexpr size_t WS_QKVM = 122ull << 20;
constexpr size_t WS_ACC = 140ull << 20;

__device__ __forceinline__ float bf2f(u16 v) { return __uint_as_float(((unsigned)v) << 16); }
__device__ __forceinline__ u16 f2bf(float f) {
  __hip_bfloat16 h = __float2bfloat16(f);
  return *reinterpret_cast<u16*>(&h);
}
__device__ __forceinline__ float rbf(float f) { return bf2f(f2bf(f)); }
__device__ __forceinline__ unsigned pack2(float a, float b) {
  unsigned r = (unsigned)f2bf(a) | ((unsigned)f2bf(b) << 16);
  return r == 0xFFFFFFFFu ? 0x7FFF7FFFu : r;
}

__global__ __launch_bounds__(256) void ln_kernel(const u16* __restrict__ x, const u16* __restrict__ mem0,
                                                  u16* __restrict__ xn, u16* memx, u16* qkvm, float* part, float* acc) {
  int wave = threadIdx.x >> 6, lane = threadIdx.x & 63;
  int row = blockIdx.x * 4 + wave;
  const u16* xr = x + (size_t)row * DM;
  float v[16];
  bf16x8 a = *reinterpret_cast<const bf16x8*>(xr + lane * 8);
  bf16x8 b = *reinterpret_cast<const bf16x8*>(xr + 512 + lane * 8);
  float s = 0.f;
  for (int i = 0; i < 8; i++) { v[i] = bf2f((u16)a[i]); v[8 + i] = bf2f((u16)b[i]); s += v[i] + v[8 + i]; }
  for (int o = 32; o; o >>= 1) s += __shfl_xor(s, o);
  float m = s * (1.f / DM);
  float q = 0.f;
  for (int i = 0; i < 16; i++) { float d = v[i] - m; q += d * d; }
  for (int o = 32; o; o >>= 1) q += __shfl_xor(q, o);
  float r = rsqrtf(q * (1.f / DM) + 1e-5f);
  bf16x8 oa, ob;
  for (int i = 0; i < 8; i++) { oa[i] = (short)f2bf((v[i] - m) * r); ob[i] = (short)f2bf((v[8 + i] - m) * r); }
  *reinterpret_cast<bf16x8*>(xn + (size_t)row * DM + lane * 8) = oa;
  *reinterpret_cast<bf16x8*>(xn + (size_t)row * DM + 512 + lane * 8) = ob;
  size_t tid = (size_t)blockIdx.x * 256 + threadIdx.x, nth = (size_t)gridDim.x * 256;
  u32x4 P = {0xFFFFFFFFu, 0xFFFFFFFFu, 0xFFFFFFFFu, 0xFFFFFFFFu};
  for (size_t i = tid; i < (size_t)NSEG * MM * DM / 8; i += nth) {
    u32x4 val = P;
    if (i < MM * DM / 8) { int blk = (int)i >> 3, rw = (int)i & 7; val = reinterpret_cast<const u32x4*>(mem0)[rw * 128 + blk]; }
    reinterpret_cast<u32x4*>(memx)[i] = val;
  }
  for (size_t i = tid; i < (size_t)NSEG * MM * NQKV / 8; i += nth) reinterpret_cast<u32x4*>(qkvm)[i] = P;
  for (size_t i = tid; i < WS_PART_BYTES / 16; i += nth) reinterpret_cast<u32x4*>(part)[i] = P;
  for (size_t i = tid; i < (size_t)S_ * DM / 4; i += nth) reinterpret_cast<u32x4*>(acc)[i] = u32x4{0u, 0u, 0u, 0u};
}

constexpr int BN = 128, BK = 64;
constexpr int B_BYTES = BK * BN * 2;

__device__ __forceinline__ float gelu_tanh(float x) {
  float u = 0.7978845608028654f * (x + 0.044715f * x * x * x);
  float t = 1.f - 2.f / (__expf(2.f * u) + 1.f);
  return 0.5f * x * (1.f + t);
}
__device__ __forceinline__ void st8_sc1(void* p, bf16x4 v) {
  asm volatile("global_store_dwordx2 %0, %1, off sc1\n\ts_nop 1" ::"v"(p), "v"(v) : "memory");
}

template <int BMT>
__device__ __forceinline__ void gemm_tile(const int EPI, char* smem, const u16* __restrict__ A, int lda, const u16* __restrict__ B, int ldb,
                                          int m0, int bcol, int k0, int nkt, void* out, int ldo, int ocol, const u16* bias) {
  constexpr int A_BYTES = BMT * BK * 2, STAGE = A_BYTES + B_BYTES;
  constexpr int WN = BMT == 256 ? 2 : 4, NT = BMT == 256 ? 4 : 2;
  int tid = threadIdx.x;
  asm volatile("" : "+v"(tid));
  const int wid = __builtin_amdgcn_readfirstlane(tid >> 6), lane = tid & 63, fr = lane & 15, fq = lane >> 4;
  const int wr = wid / WN, wc = wid % WN;
  auto stage = [&](int buf, int kt) {
    char* sa = smem + buf * STAGE;
    char* sb = sa + A_BYTES;
    int kk = k0 + kt * BK;
#pragma unroll
    for (int i = 0; i < BMT / 64; i++) {
      int pb = (wid * (BMT / 64) + i) * 1024 + lane * 16;
      int row = pb >> 7, chs = (pb >> 4) & 7, ch = chs ^ ((row >> 1) & 7);
      __builtin_amdgcn_global_load_lds((const unsigned*)(A + (size_t)(m0 + row) * lda + kk + ch * 8),
                                       (unsigned*)(sa + (wid * (BMT / 64) + i) * 1024), 16, 0, 0);
    }
#pragma unroll
    for (int i = 0; i < 2; i++) {
      int pb = (wid * 2 + i) * 1024 + lane * 16;
      int row = pb >> 8, chs = (pb >> 4) & 15, ch = chs ^ (((row & 3) << 2) | ((row >> 2) & 3));
      __builtin_amdgcn_global_load_lds((const unsigned*)(B + (size_t)(kk + row) * ldb + bcol + ch * 8),
                                       (unsigned*)(sb + (wid * 2 + i) * 1024), 16, 0, 0);
    }
  };
  f32x4 acc[4][NT];
#pragma unroll
  for (int i = 0; i < 4; i++)
#pragma unroll
    for (int j = 0; j < NT; j++) acc[i][j] = f32x4{0.f, 0.f, 0.f, 0.f};
  __syncthreads();
  stage(0, 0);
  for (int t = 0; t < nkt; t++) {
    __syncthreads();
    if (t + 1 < nkt) stage((t + 1) & 1, t + 1);
    char* sa = smem + (t & 1) * STAGE;
    char* sb = sa + A_BYTES;
#pragma unroll
    for (int ks = 0; ks < 2; ks++) {
      bf16x8 af[4], bfr[NT];
#pragma unroll
      for (int m = 0; m < 4; m++) {
        int row = wr * 64 + m * 16 + fr, ch = ks * 4 + fq;
        af[m] = *reinterpret_cast<const bf16x8*>(sa + row * 128 + 16 * (ch ^ ((row >> 1) & 7)));
      }
#pragma unroll
      for (int n = 0; n < NT; n++) {
        int c = wc * NT + n;
        int q = fr >> 2, pp = fr & 3;
        bf16x4 lo, hi;
        {
          int row = ks * 32 + 8 * fq + q, ch = 2 * c + (pp >> 1);
          int off = 256 * row + 16 * (ch ^ (((row & 3) << 2) | ((row >> 2) & 3))) + 8 * (pp & 1);
          lo = __builtin_amdgcn_ds_read_tr16_b64_v4i16((lds_b4*)(sb + off));
        }
        {
          int row = ks * 32 + 8 * fq + 4 + q, ch = 2 * c + (pp >> 1);
          int off = 256 * row + 16 * (ch ^ (((row & 3) << 2) | ((row >> 2) & 3))) + 8 * (pp & 1);
          hi = __builtin_amdgcn_ds_read_tr16_b64_v4i16((lds_b4*)(sb + off));
        }
        bfr[n] = bf16x8{lo[0], lo[1], lo[2], lo[3], hi[0], hi[1], hi[2], hi[3]};
      }
#pragma unroll
      for (int m = 0; m < 4; m++)
#pragma unroll
        for (int n = 0; n < NT; n++)
          acc[m][n] = __builtin_amdgcn_mfma_f32_16x16x32_bf16(bfr[n], af[m], acc[m][n], 0, 0, 0);
    }
  }
#pragma unroll
  for (int m = 0; m < 4; m++) {
    int row = m0 + wr * 64 + m * 16 + fr;
#pragma unroll
    for (int n = 0; n < NT; n++) {
      int col = ocol + (wc * NT + n) * 16 + fq * 4;
      if (EPI == 0 || EPI == 1) {
        bf16x4 o;
#pragma unroll
        for (int r = 0; r < 4; r++) o[r] = (short)f2bf(acc[m][n][r]);
        u16* dst = (u16*)out + (size_t)row * ldo + col;
        if (EPI == 0) *reinterpret_cast<bf16x4*>(dst) = o; else st8_sc1(dst, o);
      } else if (EPI == 2) {
        bf16x4 o, bb = *reinterpret_cast<const bf16x4*>(bias + col);
#pragma unroll
        for (int r = 0; r < 4; r++) o[r] = (short)f2bf(gelu_tanh(rbf(rbf(acc[m][n][r]) + bf2f((u16)bb[r]))));
        st8_sc1((u16*)out + (size_t)row * ldo + col, o);
      } else {
        float* dst = (float*)out + (size_t)row * ldo + col;
#pragma unroll
        for (int r = 0; r < 4; r++) atomicAdd(dst + r, acc[m][n][r]);
      }
    }
  }
}

__global__ __launch_bounds__(512, 2) void kv_kernel(const u16* __restrict__ xn, const u16* __restrict__ Wk, const u16* __restrict__ Wv, u16* qkv) {
  extern __shared__ __attribute__((aligned(16))) char smem[];
  int b = blockIdx.x, m0 = (b & 7) * 256, nt = b >> 3;
  const u16* W = nt < 32 ? Wk : Wv;
  gemm_tile<256>(0, smem, xn, DM, W, 4096, m0, (nt & 31) * 128, 0, DM / BK, qkv, NQKV, 4096 + nt * 128, nullptr);
}

constexpr int KROW = 272;
constexpr int R_RED = 0;
constexpr int R_KT = 24576;
constexpr int R_VT = R_KT + 32 * KROW;
constexpr int R_RED2 = R_VT + 32 * KROW;
constexpr int R_X = R_RED2 + 2048;
constexpr int R_STG = R_X + 6144;
constexpr int R_WO = R_STG;
constexpr int R_LDS = R_STG + 12 * 8192;
#define RED_DELAY 8
#define DLY_B 0
#define DLY_A 0
#define STAMP(i)
#define ST2(i)

__device__ __forceinline__ float xor16(float x) {
  unsigned u = __float_as_uint(x);
  auto r = __builtin_amdgcn_permlane16_swap(u, u, false, false);
  unsigned a = r[0], b = r[1];
  return __uint_as_float(((threadIdx.x >> 4) & 1) ? a : b);
}
__device__ __forceinline__ float xor32(float x) {
  unsigned u = __float_as_uint(x);
  auto r = __builtin_amdgcn_permlane32_swap(u, u, false, false);
  unsigned a = r[0], b = r[1];
  return __uint_as_float(((threadIdx.x >> 5) & 1) ? a : b);
}
__device__ __forceinline__ bool poisoned(const u32x4& v) {
  return (v[0] == 0xFFFFFFFFu) | (v[1] == 0xFFFFFFFFu) | (v[2] == 0xFFFFFFFFu) | (v[3] == 0xFFFFFFFFu);
}
using u32x2 = __attribute__((ext_vector_type(2))) unsigned;

__device__ __forceinline__ void rec_role(char* smem, const u16* __restrict__ Wq, const u16* __restrict__ Wk,
                                         const u16* __restrict__ Wv, const u16* __restrict__ Wo,
                                         const u16* __restrict__ qkvt, u16* memx, u16* qkvm,
                                         float* part, unsigned* tmo) {
  const int tid = threadIdx.x, w = __builtin_amdgcn_readfirstlane(tid >> 6), lane = tid & 63, fr = lane & 15, g = lane >> 4;
  const int rb = blockIdx.x;
  const int h = rb >> 2, j = rb & 3;
  const unsigned spin_cap = 4000000;
  char* stg = smem + R_STG + w * 8192;
  if (tid == 0) { *reinterpret_cast<volatile unsigned*>(smem + R_RED2 + 1024) = 0u; *reinterpret_cast<volatile unsigned*>(smem + R_RED2 + 1028) = 0u; }
  auto stage_tokens = [&](int sn) {
    if ((sn & 7) == 0) {
      unsigned spins = 0;
      while (__hip_atomic_load((gu32*)(tmo + C_KV + (sn >> 3) * 32), __ATOMIC_RELAXED, __HIP_MEMORY_SCOPE_AGENT) < 64u) {
        __builtin_amdgcn_s_sleep(8);
        if (++spins > 40000000u) { __hip_atomic_store((gu32*)tmo, 7u, __ATOMIC_RELAXED, __HIP_MEMORY_SCOPE_AGENT); break; }
      }
    }
    int t0 = tid - 640;
    asm volatile("" : "+v"(t0));
#pragma unroll
    for (int i = 0; i < 8; i += 2) {
      int c = t0 + 128 * i, c1 = c + 128;
      const u16* s0 = qkvt + (size_t)(sn * SEG + ((c >> 4) & 31)) * NQKV + 4096 * (1 + (c >> 9)) + h * 128 + (c & 15) * 8;
      const u16* s1 = qkvt + (size_t)(sn * SEG + ((c1 >> 4) & 31)) * NQKV + 4096 * (1 + (c1 >> 9)) + h * 128 + (c1 & 15) * 8;
      u32x4 va, vb;
      asm volatile("global_load_dwordx4 %0, %2, off sc1\n\tglobal_load_dwordx4 %1, %3, off sc1\n\ts_waitcnt vmcnt(0)"
                   : "=&v"(va), "=&v"(vb) : "v"(s0), "v"(s1) : "memory");
      *reinterpret_cast<u32x4*>(smem + ((c >> 9) ? R_VT : R_KT) + ((c >> 4) & 31) * KROW + (c & 15) * 16) = va;
      *reinterpret_cast<u32x4*>(smem + ((c1 >> 9) ? R_VT : R_KT) + ((c1 >> 4) & 31) * KROW + (c1 & 15) * 16) = vb;
    }
  };

  if (w < 4) {
    __builtin_amdgcn_s_setprio(3);
    bf16x8 wo[4][4];
#pragma unroll
    for (int half = 0; half < 2; half++) {
#pragma unroll
      for (int i = 0; i < 8; i++) {
        int c = i * 64 + lane, row = c >> 2, ch = c & 3;
        u32x4 v = *reinterpret_cast<const u32x4*>(Wo + (size_t)(h * 128 + row) * DM + 256 * j + 64 * w + 32 * half + ch * 8);
        *reinterpret_cast<u32x4*>(stg + row * 64 + ch * 16) = v;
      }
      __builtin_amdgcn_s_waitcnt(0);
      __builtin_amdgcn_wave_barrier();
#pragma unroll
      for (int t2 = 0; t2 < 2; t2++)
#pragma unroll
        for (int kk = 0; kk < 4; kk++) {
          int q = fr >> 2, pp = fr & 3;
          bf16x4 lo = __builtin_amdgcn_ds_read_tr16_b64_v4i16((lds_b4*)(stg + (kk * 32 + 4 * g + q) * 64 + t2 * 32 + pp * 8));
          bf16x4 hi = __builtin_amdgcn_ds_read_tr16_b64_v4i16((lds_b4*)(stg + (kk * 32 + 16 + 4 * g + q) * 64 + t2 * 32 + pp * 8));
          wo[half * 2 + t2][kk] = bf16x8{lo[0], lo[1], lo[2], lo[3], hi[0], hi[1], hi[2], hi[3]};
        }
      __builtin_amdgcn_s_waitcnt(0);
      __builtin_amdgcn_wave_barrier();
    }
    __syncthreads();
#pragma unroll
    for (int t4 = 0; t4 < 4; t4++)
#pragma unroll
      for (int kk = 0; kk < 4; kk++) *reinterpret_cast<bf16x8*>(smem + R_WO + w * 16384 + ((t4 * 4 + kk) * 64 + lane) * 16) = wo[t4][kk];
    for (int s = 0; s < NSEG; s++) {
      __syncthreads();
      bf16x8 kt[2][4], vt[8];
      {
        const char* kb = smem + R_KT;
        const char* vb = smem + R_VT;
#pragma unroll
        for (int t = 0; t < 2; t++)
#pragma unroll
          for (int ks = 0; ks < 4; ks++) kt[t][ks] = *reinterpret_cast<const bf16x8*>(kb + (16 * t + fr) * KROW + ks * 64 + g * 16);
        int q = fr >> 2, pp = fr & 3;
#pragma unroll
        for (int et = 0; et < 8; et++) {
          bf16x4 lo = __builtin_amdgcn_ds_read_tr16_b64_v4i16((lds_b4*)(vb + (4 * g + q) * KROW + et * 32 + pp * 8));
          bf16x4 hi = __builtin_amdgcn_ds_read_tr16_b64_v4i16((lds_b4*)(vb + (16 + 4 * g + q) * KROW + et * 32 + pp * 8));
          vt[et] = bf16x8{lo[0], lo[1], lo[2], lo[3], hi[0], hi[1], hi[2], hi[3]};
        }
      }
      STAMP(3)
      __syncthreads();
      u32x4 q0, q1, q2, q3, k0, k1, k2, k3;
      u32x2 v0, v1, v2, v3, v4, v5, v6, v7;
      {
        const char* qp = smem + R_X + ((g >> 1) * 128 + (fr & 7) * 16 + (g & 1) * 8) * 2;
        const char* vp = smem + R_X + (512 + fr * 8 + (g & 1) * 4) * 2;
        q0 = *reinterpret_cast<const u32x4*>(qp); q1 = *reinterpret_cast<const u32x4*>(qp + 1536);
        q2 = *reinterpret_cast<const u32x4*>(qp + 3072); q3 = *reinterpret_cast<const u32x4*>(qp + 4608);
        k0 = *reinterpret_cast<const u32x4*>(qp + 512); k1 = *reinterpret_cast<const u32x4*>(qp + 2048);
        k2 = *reinterpret_cast<const u32x4*>(qp + 3584); k3 = *reinterpret_cast<const u32x4*>(qp + 5120);
        v0 = *reinterpret_cast<const u32x2*>(vp); v1 = *reinterpret_cast<const u32x2*>(vp + 256);
        v2 = *reinterpret_cast<const u32x2*>(vp + 1536); v3 = *reinterpret_cast<const u32x2*>(vp + 1792);
        v4 = *reinterpret_cast<const u32x2*>(vp + 3072); v5 = *reinterpret_cast<const u32x2*>(vp + 3328);
        v6 = *reinterpret_cast<const u32x2*>(vp + 4608); v7 = *reinterpret_cast<const u32x2*>(vp + 4864);
        asm volatile("" : "+v"(q0), "+v"(q1), "+v"(q2), "+v"(q3), "+v"(k0), "+v"(k1), "+v"(k2), "+v"(k3),
                     "+v"(v0), "+v"(v1), "+v"(v2), "+v"(v3), "+v"(v4), "+v"(v5), "+v"(v6), "+v"(v7));
      }
      STAMP(4)
      f32x4 st[3];
      {
        bf16x8 qf[4] = {__builtin_bit_cast(bf16x8, q0), __builtin_bit_cast(bf16x8, q1), __builtin_bit_cast(bf16x8, q2), __builtin_bit_cast(bf16x8, q3)};
        bf16x8 km[4] = {__builtin_bit_cast(bf16x8, k0), __builtin_bit_cast(bf16x8, k1), __builtin_bit_cast(bf16x8, k2), __builtin_bit_cast(bf16x8, k3)};
#pragma unroll
        for (int t = 0; t < 3; t++) st[t] = f32x4{0.f, 0.f, 0.f, 0.f};
#pragma unroll
        for (int ks = 0; ks < 4; ks++) {
          st[0] = __builtin_amdgcn_mfma_f32_16x16x32_bf16(kt[0][ks], qf[ks], st[0], 0, 0, 0);
          st[1] = __builtin_amdgcn_mfma_f32_16x16x32_bf16(kt[1][ks], qf[ks], st[1], 0, 0, 0);
          st[2] = __builtin_amdgcn_mfma_f32_16x16x32_bf16(km[ks], qf[ks], st[2], 0, 0, 0);
        }
      }
      bf16x8 wf[4][4];
      asm volatile("" ::: "memory");
#pragma unroll
      for (int t4 = 0; t4 < 4; t4++)
#pragma unroll
        for (int kk = 0; kk < 4; kk++) wf[t4][kk] = *reinterpret_cast<const bf16x8*>(smem + R_WO + w * 16384 + ((t4 * 4 + kk) * 64 + lane) * 16);
      asm volatile("" ::: "memory");
      if (g >= 2) st[2] = f32x4{-3.0e38f, -3.0e38f, -3.0e38f, -3.0e38f};
      float mx = fmaxf(fmaxf(fmaxf(st[0][0], st[0][1]), fmaxf(st[0][2], st[0][3])), fmaxf(fmaxf(st[1][0], st[1][1]), fmaxf(st[1][2], st[1][3])));
      mx = fmaxf(mx, fmaxf(fmaxf(st[2][0], st[2][1]), fmaxf(st[2][2], st[2][3])));
      mx = fmaxf(mx, xor16(mx));
      mx = fmaxf(mx, xor32(mx));
      const float c2 = SCALE * 1.4426950408889634f;
      float mc = mx * c2;
      float sum = 0.f;
#pragma unroll
      for (int t = 0; t < 3; t++)
#pragma unroll
        for (int r = 0; r < 4; r++) {
          float e = __builtin_amdgcn_exp2f(__builtin_fmaf(st[t][r], c2, -mc));
          st[t][r] = e;
          sum += e;
        }
      sum += xor16(sum);
      sum += xor32(sum);
      float inv = __builtin_amdgcn_rcpf(sum);
      bf16x8 pf[2];
#pragma unroll
      for (int r = 0; r < 4; r++) {
        pf[0][r] = (short)f2bf(st[0][r] * inv);
        pf[0][4 + r] = (short)f2bf(st[1][r] * inv);
        pf[1][r] = (short)f2bf(st[2][r] * inv);
        pf[1][4 + r] = 0;
      }
      bf16x8 of[4];
      {
        u32x2 vv[8] = {v0, v1, v2, v3, v4, v5, v6, v7};
#pragma unroll
        for (int et = 0; et < 8; et++) {
          f32x4 o = {0.f, 0.f, 0.f, 0.f};
          o = __builtin_amdgcn_mfma_f32_16x16x32_bf16(vt[et], pf[0], o, 0, 0, 0);
          u32x4 vx = {vv[et][0], vv[et][1], 0u, 0u};
          o = __builtin_amdgcn_mfma_f32_16x16x32_bf16(__builtin_bit_cast(bf16x8, vx), pf[1], o, 0, 0, 0);
#pragma unroll
          for (int r = 0; r < 4; r++) of[et >> 1][(et & 1) * 4 + r] = (short)f2bf(o[r]);
        }
      }
      if (s + 1 < NSEG) {
        f32x4 pa[4];
#pragma unroll
        for (int t4 = 0; t4 < 4; t4++) pa[t4] = f32x4{0.f, 0.f, 0.f, 0.f};
#pragma unroll
        for (int kk = 0; kk < 4; kk++)
#pragma unroll
          for (int t4 = 0; t4 < 4; t4++) pa[t4] = __builtin_amdgcn_mfma_f32_16x16x32_bf16(of[kk], wf[t4][kk], pa[t4], 0, 0, 0);
        if (g < 2) {
#pragma unroll
          for (int t4 = 0; t4 < 4; t4++) {
            int col = 64 * w + 16 * t4 + fr;
            int dst = col >> 3, cc = col & 7;
            float* base = part + ((((size_t)(s & 3) * 4 + j) * 32 + dst) * 32 + h) * 64 + cc * 8 + 4 * g;
            u32x4 vv;
#pragma unroll
            for (int r = 0; r < 4; r++) { unsigned u = __float_as_uint(pa[t4][r]); vv[r] = u == 0xFFFFFFFFu ? 0x7FFFFFFFu : u; }
            asm volatile("global_store_dwordx4 %0, %1, off sc1\n\ts_nop 1" :: "v"(base), "v"(vv) : "memory");
          }
        }
      }
      STAMP(5)
    }
  } else {
    const int gw = w - 4;
    bf16x8 wb[6][4];
#pragma unroll
    for (int mat = 0; mat < 3; mat++) {
      const u16* W = mat == 0 ? Wq : (mat == 1 ? Wk : Wv);
#pragma unroll
      for (int i = 0; i < 8; i++) {
        int c = i * 64 + lane, row = c >> 2, ch = c & 3;
        u32x4 v = *reinterpret_cast<const u32x4*>(W + (size_t)(128 * gw + row) * 4096 + h * 128 + 32 * j + ch * 8);
        *reinterpret_cast<u32x4*>(stg + row * 64 + ch * 16) = v;
      }
      __builtin_amdgcn_s_waitcnt(0);
      __builtin_amdgcn_wave_barrier();
#pragma unroll
      for (int t2 = 0; t2 < 2; t2++)
#pragma unroll
        for (int ks = 0; ks < 4; ks++) {
          int q = fr >> 2, pp = fr & 3;
          bf16x4 lo = __builtin_amdgcn_ds_read_tr16_b64_v4i16((lds_b4*)(stg + (ks * 32 + 8 * g + q) * 64 + t2 * 32 + pp * 8));
          bf16x4 hi = __builtin_amdgcn_ds_read_tr16_b64_v4i16((lds_b4*)(stg + (ks * 32 + 8 * g + 4 + q) * 64 + t2 * 32 + pp * 8));
          wb[mat * 2 + t2][ks] = bf16x8{lo[0], lo[1], lo[2], lo[3], hi[0], hi[1], hi[2], hi[3]};
        }
      __builtin_amdgcn_s_waitcnt(0);
      __builtin_amdgcn_wave_barrier();
    }
    if (gw >= 6) stage_tokens(0);
    __syncthreads();
    for (int s = 0; s < NSEG; s++) {
      if (s > 0) { volatile unsigned* pflag = reinterpret_cast<volatile unsigned*>(smem + R_RED2 + 1028); while (*pflag != (unsigned)s) __builtin_amdgcn_s_sleep(2); }
      if (gw == 0) { ST2(0) }
      for (int i = 0; i < DLY_A; i++) __builtin_amdgcn_s_sleep(1);
      u32x4 a0, a1, a2, a3;
      {
        const u16* mp = memx + (size_t)s * MM * DM + (16 * gw + g) * 64 + (fr & 7) * 8;
        unsigned spins = 0;
        while (true) {
          asm volatile(
              "global_load_dwordx4 %0, %4, off sc1\n\t"
              "global_load_dwordx4 %1, %4, off offset:512 sc1\n\t"
              "global_load_dwordx4 %2, %4, off offset:1024 sc1\n\t"
              "global_load_dwordx4 %3, %4, off offset:1536 sc1\n\t"
              "s_waitcnt vmcnt(0)"
              : "=&v"(a0), "=&v"(a1), "=&v"(a2), "=&v"(a3) : "v"(mp) : "memory");
          bool bad = poisoned(a0) | poisoned(a1) | poisoned(a2) | poisoned(a3);
          if (!__any(bad)) break;
          if (++spins > spin_cap) { if (lane == 0) __hip_atomic_store((gu32*)tmo, 1u, __ATOMIC_RELAXED, __HIP_MEMORY_SCOPE_AGENT); break; }
        }
      }
      if (gw == 0) { ST2(1) }
      {
        bf16x8 af[4] = {__builtin_bit_cast(bf16x8, a0), __builtin_bit_cast(bf16x8, a1), __builtin_bit_cast(bf16x8, a2), __builtin_bit_cast(bf16x8, a3)};
        f32x4 acc[6];
#pragma unroll
        for (int t = 0; t < 6; t++) acc[t] = f32x4{0.f, 0.f, 0.f, 0.f};
#pragma unroll
        for (int ks = 0; ks < 4; ks++)
#pragma unroll
          for (int t = 0; t < 6; t++) acc[t] = __builtin_amdgcn_mfma_f32_16x16x32_bf16(af[ks], wb[t][ks], acc[t], 0, 0, 0);
        if (g < 2) {
          float* red = reinterpret_cast<float*>(smem + R_RED) + gw * 768;
#pragma unroll
          for (int t = 0; t < 4; t++)
#pragma unroll
            for (int r = 0; r < 4; r++) red[t * 128 + (4 * g + r) * 16 + fr] = acc[t][r];
#pragma unroll
          for (int t = 4; t < 6; t++) *reinterpret_cast<f32x4*>(red + t * 128 + fr * 8 + 4 * g) = acc[t];
        }
      }
      if (gw == 0) { ST2(2) }
      __syncthreads();
      if (gw < 2) {
        if (gw == 0 || lane < 32) {
          int off = gw * 512 + lane * 8;
          const float* red = reinterpret_cast<const float*>(smem + R_RED) + off;
          f32x4 x0 = {0.f, 0.f, 0.f, 0.f}, x1 = x0;
#pragma unroll
          for (int ww = 0; ww < 8; ww++) {
            x0 += *reinterpret_cast<const f32x4*>(red + ww * 768);
            x1 += *reinterpret_cast<const f32x4*>(red + ww * 768 + 4);
          }
          u32x4 v4 = {pack2(x0[0], x0[1]), pack2(x0[2], x0[3]), pack2(x1[0], x1[1]), pack2(x1[2], x1[3])};
          u16* dst = qkvm + (((size_t)s * NH + h) * 4 + j) * 768 + off;
          asm volatile("global_store_dwordx4 %0, %1, off sc1\n\ts_nop 1" ::"v"(dst), "v"(v4) : "memory");
        }
      } else {
        int c = tid - 384;
        const u16* src = qkvm + ((size_t)s * NH + h) * 3072 + c * 8;
        unsigned spins = 0;
        for (int i = 0; i < DLY_B; i++) __builtin_amdgcn_s_sleep(1);
        u32x4 v;
        while (true) {
          asm volatile("global_load_dwordx4 %0, %1, off sc1\n\ts_waitcnt vmcnt(0)" : "=&v"(v) : "v"(src) : "memory");
          if (!__any(poisoned(v))) break;
          if (++spins > spin_cap) { if (lane == 0) __hip_atomic_store((gu32*)tmo, 2u, __ATOMIC_RELAXED, __HIP_MEMORY_SCOPE_AGENT); break; }
        }
        *reinterpret_cast<u32x4*>(smem + R_X + c * 16) = v;
      }
      __syncthreads();
      if (gw < 2) {
      } else if (gw < 4) {
        if (s > 0) {
          int ln = lane; asm volatile("" : "+v"(ln));
          float* base = part + ((((size_t)((s - 1) & 3) * 4 + j) * 32 + h) * 32 + 16 * (gw - 2)) * 64 + ln * 4;
          u32x4 P = {0xFFFFFFFFu, 0xFFFFFFFFu, 0xFFFFFFFFu, 0xFFFFFFFFu};
          asm volatile(
              "global_store_dwordx4 %0, %1, off sc1\n\t"
              "global_store_dwordx4 %0, %1, off offset:1024 sc1\n\t"
              "global_store_dwordx4 %0, %1, off offset:2048 sc1\n\t"
              "global_store_dwordx4 %0, %1, off offset:3072 sc1\n\ts_nop 1" ::"v"(base), "v"(P) : "memory");
        }
      } else if (gw < 6) {
        if (s + 1 < NSEG && gw == 4) {
          int ln = lane; asm volatile("" : "+v"(ln));
          float* base = part + ((((size_t)(s & 3) * 4 + j) * 32 + h) * 32) * 64 + ln * 4;
          unsigned spins = 0;
          u32x4 b0, b1, b2, b3, b4, b5, b6, b7;
          for (int i = 0; i < RED_DELAY; i++) __builtin_amdgcn_s_sleep(1);
          ST2(6)
          while (true) {
            asm volatile(
                "global_load_dwordx4 %0, %8, off sc1\n\t"
                "global_load_dwordx4 %1, %8, off offset:1024 sc1\n\t"
                "global_load_dwordx4 %2, %8, off offset:2048 sc1\n\t"
                "global_load_dwordx4 %3, %8, off offset:3072 sc1\n\t"
                "global_load_dwordx4 %4, %9, off sc1\n\t"
                "global_load_dwordx4 %5, %9, off offset:1024 sc1\n\t"
                "global_load_dwordx4 %6, %9, off offset:2048 sc1\n\t"
                "global_load_dwordx4 %7, %9, off offset:3072 sc1\n\t"
                "s_waitcnt vmcnt(0)"
                : "=&v"(b0), "=&v"(b1), "=&v"(b2), "=&v"(b3), "=&v"(b4), "=&v"(b5), "=&v"(b6), "=&v"(b7) : "v"(base), "v"(base + 1024) : "memory");
            bool bad = poisoned(b0) | poisoned(b1) | poisoned(b2) | poisoned(b3) | poisoned(b4) | poisoned(b5) | poisoned(b6) | poisoned(b7);
            if (!__any(bad)) break;
            if (++spins > spin_cap) { if (lane == 0) __hip_atomic_store((gu32*)tmo, 3u, __ATOMIC_RELAXED, __HIP_MEMORY_SCOPE_AGENT); break; }
          }
          ST2(7)
          f32x4 val = ((__builtin_bit_cast(f32x4, b0) + __builtin_bit_cast(f32x4, b1)) + (__builtin_bit_cast(f32x4, b2) + __builtin_bit_cast(f32x4, b3))) +
                      ((__builtin_bit_cast(f32x4, b4) + __builtin_bit_cast(f32x4, b5)) + (__builtin_bit_cast(f32x4, b6) + __builtin_bit_cast(f32x4, b7)));
#pragma unroll
          for (int i = 0; i < 4; i++) { val[i] += xor16(val[i]); val[i] += xor32(val[i]); }
          float* r2 = reinterpret_cast<float*>(smem + R_RED2);
          if (lane < 16) {
#pragma unroll
            for (int i = 0; i < 4; i++) r2[(4 * (fr & 1) + i) * 8 + (fr >> 1)] = val[i];
          }
          __builtin_amdgcn_s_waitcnt(0xc07f);
          __builtin_amdgcn_wave_barrier();
          if (lane < 8) {
            const float* q2 = reinterpret_cast<const float*>(smem + R_RED2) + lane * 8;
            f32x4 x0 = *reinterpret_cast<const f32x4*>(q2);
            f32x4 x1 = *reinterpret_cast<const f32x4*>(q2 + 4);
            u32x4 v4 = {pack2(x0[0], x0[1]), pack2(x0[2], x0[3]), pack2(x1[0], x1[1]), pack2(x1[2], x1[3])};
            u16* dst = memx + (size_t)(s + 1) * MM * DM + (32 * j + h) * 64 + ln * 8;
            asm volatile("global_store_dwordx4 %0, %1, off sc1\n\ts_nop 1" ::"v"(dst), "v"(v4) : "memory");
          }
          if (lane == 0) *reinterpret_cast<volatile unsigned*>(smem + R_RED2 + 1028) = (unsigned)(s + 1);
        }
      } else {
        if (s + 1 < NSEG) stage_tokens(s + 1);
      }
    }
  }
}

struct MegaP {
  const u16* x; const u16* Wq; const u16* Wk; const u16* Wv; const u16* Wo; const u16* w1; const u16* b1; const u16* w2; const u16* b2;
  u16* xn; u16* qkv; u16* H; u16* O; u16* memx; u16* qkvm; float* part; float* acc; unsigned* ctl; u16* out;
};

__device__ __forceinline__ void wait_count(unsigned* word, unsigned target, unsigned* tmo) {
  if (threadIdx.x == 0) {
    unsigned spins = 0;
    while (__hip_atomic_load((gu32*)word, __ATOMIC_RELAXED, __HIP_MEMORY_SCOPE_AGENT) < target) {
      __builtin_amdgcn_s_sleep(8);
      if (++spins > 40000000u) { __hip_atomic_store((gu32*)tmo, 9u, __ATOMIC_RELAXED, __HIP_MEMORY_SCOPE_AGENT); break; }
    }
    __builtin_amdgcn_fence(__ATOMIC_ACQUIRE, "agent");
    asm volatile("s_waitcnt vmcnt(0)" ::: "memory");
  }
  __syncthreads();
}
__device__ __forceinline__ unsigned signal_count(unsigned* word, char* smem) {
  asm volatile("s_waitcnt vmcnt(0)" ::: "memory");
  __syncthreads();
  volatile unsigned* bc = reinterpret_cast<volatile unsigned*>(smem + 150 * 1024);
  if (threadIdx.x == 0) *bc = __hip_atomic_fetch_add((gu32*)word, 1u, __ATOMIC_RELAXED, __HIP_MEMORY_SCOPE_AGENT);
  __syncthreads();
  return *bc;
}

__device__ __forceinline__ void final_rows(const MegaP& p, int mt) {
  int tid = threadIdx.x;
  asm volatile("" : "+v"(tid));
  for (int i = 0; i < 64; i++) {
    size_t e = (size_t)mt * 128 * DM + ((size_t)i * 512 + tid) * 4;
    f32x4 a;
    asm volatile("global_load_dwordx4 %0, %1, off sc1\n\ts_waitcnt vmcnt(0)" : "=&v"(a) : "v"(p.acc + e) : "memory");
    bf16x4 xv = *reinterpret_cast<const bf16x4*>(p.x + e);
    bf16x4 bv = *reinterpret_cast<const bf16x4*>(p.b2 + (e & (DM - 1)));
    bf16x4 o;
#pragma unroll
    for (int r = 0; r < 4; r++) o[r] = (short)f2bf(bf2f((u16)xv[r]) + a[r] + bf2f((u16)bv[r]));
    *reinterpret_cast<bf16x4*>(p.out + e) = o;
  }
}

__device__ __forceinline__ void attn_task(char* smem, const MegaP& p, int s, int hg) {
  int tid = threadIdx.x;
  asm volatile("" : "+v"(tid));
  const int w = __builtin_amdgcn_readfirstlane(tid >> 6), lane = tid & 63, fr = lane & 15, g = lane >> 4;
  const int h = hg * 8 + w;
  char* sV = smem + w * 32 * KROW;
  const u16* tok = p.qkv + (size_t)s * SEG * NQKV + h * 128;
#pragma unroll
  for (int i = 0; i < 8; i++) {
    int c = i * 64 + lane, row = c >> 4, ch = c & 15;
    *reinterpret_cast<u32x4*>(sV + row * KROW + ch * 16) = *reinterpret_cast<const u32x4*>(tok + (size_t)row * NQKV + 8192 + ch * 8);
  }
  bf16x8 kt[2][4];
#pragma unroll
  for (int t = 0; t < 2; t++)
#pragma unroll
    for (int ks = 0; ks < 4; ks++) kt[t][ks] = *reinterpret_cast<const bf16x8*>(tok + (size_t)(16 * t + fr) * NQKV + 4096 + ks * 32 + g * 8);
  u32x4 k0, k1, k2, k3;
  u32x2 v0, v1, v2, v3, v4, v5, v6, v7;
  {
    const u16* eb = p.qkvm + ((size_t)s * NH + h) * 3072;
    const u16* qp = eb + (g >> 1) * 128 + (fr & 7) * 16 + (g & 1) * 8;
    const u16* vp = eb + 512 + fr * 8 + (g & 1) * 4;
    unsigned spins = 0;
    while (true) {
      asm volatile(
          "global_load_dwordx4 %0, %12, off offset:512 sc1\n\t"
          "global_load_dwordx4 %1, %12, off offset:2048 sc1\n\t"
          "global_load_dwordx4 %2, %12, off offset:3584 sc1\n\t"
          "global_load_dwordx4 %3, %13, off offset:1024 sc1\n\t"
          "global_load_dwordx2 %4, %14, off sc1\n\t"
          "global_load_dwordx2 %5, %14, off offset:256 sc1\n\t"
          "global_load_dwordx2 %6, %14, off offset:1536 sc1\n\t"
          "global_load_dwordx2 %7, %14, off offset:1792 sc1\n\t"
          "global_load_dwordx2 %8, %14, off offset:3072 sc1\n\t"
          "global_load_dwordx2 %9, %14, off offset:3328 sc1\n\t"
          "global_load_dwordx2 %10, %15, off offset:512 sc1\n\t"
          "global_load_dwordx2 %11, %15, off offset:768 sc1\n\t"
          "s_waitcnt vmcnt(0)"
          : "=&v"(k0), "=&v"(k1), "=&v"(k2), "=&v"(k3), "=&v"(v0), "=&v"(v1), "=&v"(v2), "=&v"(v3), "=&v"(v4), "=&v"(v5), "=&v"(v6), "=&v"(v7)
          : "v"(qp), "v"(qp + 2048), "v"(vp), "v"(vp + 2048) : "memory");
      bool bad = poisoned(k0) | poisoned(k1) | poisoned(k2) | poisoned(k3);
      bad |= (v0[0] == 0xFFFFFFFFu) | (v0[1] == 0xFFFFFFFFu) | (v1[0] == 0xFFFFFFFFu) | (v1[1] == 0xFFFFFFFFu) |
             (v2[0] == 0xFFFFFFFFu) | (v2[1] == 0xFFFFFFFFu) | (v3[0] == 0xFFFFFFFFu) | (v3[1] == 0xFFFFFFFFu) |
             (v4[0] == 0xFFFFFFFFu) | (v4[1] == 0xFFFFFFFFu) | (v5[0] == 0xFFFFFFFFu) | (v5[1] == 0xFFFFFFFFu) |
             (v6[0] == 0xFFFFFFFFu) | (v6[1] == 0xFFFFFFFFu) | (v7[0] == 0xFFFFFFFFu) | (v7[1] == 0xFFFFFFFFu);
      if (!__any(bad)) break;
      __builtin_amdgcn_s_sleep(32);
      if (++spins > 4000000u) { if (lane == 0) __hip_atomic_store((gu32*)p.ctl, 5u, __ATOMIC_RELAXED, __HIP_MEMORY_SCOPE_AGENT); break; }
    }
  }
  bf16x8 km[4] = {__builtin_bit_cast(bf16x8, k0), __builtin_bit_cast(bf16x8, k1), __builtin_bit_cast(bf16x8, k2), __builtin_bit_cast(bf16x8, k3)};
  u32x2 vv[8] = {v0, v1, v2, v3, v4, v5, v6, v7};
  __builtin_amdgcn_s_waitcnt(0);
  __builtin_amdgcn_wave_barrier();
#pragma unroll
  for (int qt = 0; qt < 2; qt++) {
    const int qi = qt * 16 + fr;
    f32x4 st[3];
#pragma unroll
    for (int t = 0; t < 3; t++) st[t] = f32x4{0.f, 0.f, 0.f, 0.f};
#pragma unroll
    for (int ks = 0; ks < 4; ks++) {
      bf16x8 qf = *reinterpret_cast<const bf16x8*>(tok + (size_t)qi * NQKV + ks * 32 + g * 8);
      st[0] = __builtin_amdgcn_mfma_f32_16x16x32_bf16(kt[0][ks], qf, st[0], 0, 0, 0);
      st[1] = __builtin_amdgcn_mfma_f32_16x16x32_bf16(kt[1][ks], qf, st[1], 0, 0, 0);
      st[2] = __builtin_amdgcn_mfma_f32_16x16x32_bf16(km[ks], qf, st[2], 0, 0, 0);
    }
    float mx = -3.0e38f;
#pragma unroll
    for (int t = 0; t < 3; t++)
#pragma unroll
      for (int r = 0; r < 4; r++) {
        bool masked = t < 2 ? (16 * t + 4 * g + r > qi) : (g >= 2);
        float v = masked ? -3.0e38f : st[t][r];
        st[t][r] = v;
        mx = fmaxf(mx, v);
      }
    mx = fmaxf(mx, xor16(mx));
    mx = fmaxf(mx, xor32(mx));
    const float c2 = SCALE * 1.4426950408889634f;
    float mc = mx * c2, sum = 0.f;
#pragma unroll
    for (int t = 0; t < 3; t++)
#pragma unroll
      for (int r = 0; r < 4; r++) {
        float e = __builtin_amdgcn_exp2f(__builtin_fmaf(st[t][r], c2, -mc));
        st[t][r] = e;
        sum += e;
      }
    sum += xor16(sum);
    sum += xor32(sum);
    float inv = __builtin_amdgcn_rcpf(sum);
    bf16x8 pf[2];
#pragma unroll
    for (int r = 0; r < 4; r++) {
      pf[0][r] = (short)f2bf(st[0][r] * inv);
      pf[0][4 + r] = (short)f2bf(st[1][r] * inv);
      pf[1][r] = (short)f2bf(st[2][r] * inv);
      pf[1][4 + r] = 0;
    }
    int q = fr >> 2, pp = fr & 3;
    u16* op = p.O + (size_t)(s * SEG + qi) * 4096 + h * 128;
#pragma unroll
    for (int et = 0; et < 8; et++) {
      bf16x4 lo = __builtin_amdgcn_ds_read_tr16_b64_v4i16((lds_b4*)(sV + (4 * g + q) * KROW + et * 32 + pp * 8));
      bf16x4 hi = __builtin_amdgcn_ds_read_tr16_b64_v4i16((lds_b4*)(sV + (16 + 4 * g + q) * KROW + et * 32 + pp * 8));
      bf16x8 vf = bf16x8{lo[0], lo[1], lo[2], lo[3], hi[0], hi[1], hi[2], hi[3]};
      f32x4 o = {0.f, 0.f, 0.f, 0.f};
      o = __builtin_amdgcn_mfma_f32_16x16x32_bf16(vf, pf[0], o, 0, 0, 0);
      u32x4 vx = {vv[et][0], vv[et][1], 0u, 0u};
      o = __builtin_amdgcn_mfma_f32_16x16x32_bf16(__builtin_bit_cast(bf16x8, vx), pf[1], o, 0, 0, 0);
      bf16x4 ov;
#pragma unroll
      for (int r = 0; r < 4; r++) ov[r] = (short)f2bf(o[r]);
      st8_sc1(op + et * 16 + g * 4, ov);
    }
  }
}

constexpr int T_KV = 512, T_Q = T_KV + 256, T_H = T_Q + 224, T_B = T_H + 256, T_END = T_B + 16 * 48;
constexpr int MEGA_LDS = 152 * 1024;

__global__ __launch_bounds__(768, 3) void mega_kernel(MegaP p) {
  extern __shared__ __attribute__((aligned(16))) char smem[];
  if (blockIdx.x < 128) {
    rec_role(smem, p.Wq, p.Wk, p.Wv, p.Wo, p.qkv, p.memx, p.qkvm, p.part, p.ctl);
    return;
  }
  if (threadIdx.x >= 512) return;
  volatile unsigned* bc = reinterpret_cast<volatile unsigned*>(smem + 150 * 1024 + 16);
  while (true) {
    __syncthreads();
    if (threadIdx.x == 0) *bc = __hip_atomic_fetch_add((gu32*)(p.ctl + C_TICKET), 1u, __ATOMIC_RELAXED, __HIP_MEMORY_SCOPE_AGENT);
    __syncthreads();
    int t = (int)*bc;
    if (t >= T_END) break;
    if (t < T_B) {
      int epi, m, nt, k0, nkt, lda, ldb, ldo; const u16* A; const u16* B; void* out; unsigned* sig;
      int ocol_add = 0;
      if (t < T_KV) {
        m = t >> 6; int n2 = t & 63; nt = n2 & 31; epi = 1; A = p.xn; lda = DM; B = n2 < 32 ? p.Wk : p.Wv; ldb = 4096; k0 = 0; nkt = DM / BK; out = p.qkv; ldo = NQKV; sig = p.ctl + C_KV + m * 32;
        ocol_add = n2 < 32 ? 4096 : 8192;
      } else if (t < T_Q) {
        int u = t - T_KV; m = u >> 5; nt = u & 31; epi = 1; A = p.xn; lda = DM; B = p.Wq; ldb = 4096; k0 = 0; nkt = DM / BK; out = p.qkv; ldo = NQKV; sig = p.ctl + C_Q + m * 32;
      } else if (t < T_H) {
        int u = t - T_Q; m = u / 28; nt = u % 28; epi = 2; A = p.xn; lda = DM; B = p.w1; ldb = FF; k0 = 0; nkt = DM / BK; out = p.H; ldo = FF; sig = p.ctl + C_H + m * 32;
      } else {
        int u = t - T_H; m = u >> 5; nt = (u >> 2) & 7; epi = 3; A = p.H; lda = FF; B = p.w2; ldb = DM; k0 = (u & 3) * 896; nkt = 14; out = p.acc; ldo = DM; sig = p.ctl + C_F + 2 * m * 32;
        wait_count(p.ctl + C_H + m * 32, 28, p.ctl);
      }
      gemm_tile<256>(epi, smem, A, lda, B, ldb, m * 256, nt * 128, k0, nkt, out, ldo, ocol_add + nt * 128, p.b1);
      unsigned prev = signal_count(sig, smem);
      if (epi == 3) {
        if (prev == 63) final_rows(p, 2 * m);
        prev = signal_count(sig + 32, smem);
        if (prev == 63) final_rows(p, 2 * m + 1);
      }
    } else {
      int u = t - T_B, mt = u / 48, v = u % 48;
      if (v < 16) {
        int s = mt * 4 + (v >> 2), hg = v & 3;
        wait_count(p.ctl + C_KV + (mt >> 1) * 32, 64, p.ctl);
        wait_count(p.ctl + C_Q + (mt >> 1) * 32, 32, p.ctl);
        attn_task(smem, p, s, hg);
        signal_count(p.ctl + C_A + mt * 32, smem);
      } else {
        int d = v - 16, nt = d >> 2, ks = d & 3;
        wait_count(p.ctl + C_A + mt * 32, 16, p.ctl);
        gemm_tile<128>(3, smem, p.O, 4096, p.Wo, DM, mt * 128, nt * 128, ks * 1024, 16, p.acc, DM, nt * 128, nullptr);
        unsigned prev = signal_count(p.ctl + C_F + mt * 32, smem);
        if (prev == 63) final_rows(p, mt);
      }
    }
  }
}

extern "C" void kernel_launch(void* const* d_in, const int* in_sizes, int n_in, void* d_out, int out_size,
                              void* d_ws, size_t ws_size, hipStream_t stream) {
  char* ws = (char*)d_ws;
  MegaP p;
  p.x = (const u16*)d_in[0];
  const u16* mem0 = (const u16*)d_in[1];
  p.Wq = (const u16*)d_in[2]; p.Wk = (const u16*)d_in[3]; p.Wv = (const u16*)d_in[4]; p.Wo = (const u16*)d_in[5];
  p.w1 = (const u16*)d_in[8]; p.b1 = (const u16*)d_in[9]; p.w2 = (const u16*)d_in[10]; p.b2 = (const u16*)d_in[11];
  p.xn = (u16*)(ws + WS_XN); p.qkv = (u16*)(ws + WS_QKV); p.H = (u16*)(ws + WS_H); p.O = (u16*)(ws + WS_O);
  p.memx = (u16*)(ws + WS_MEM); p.qkvm = (u16*)(ws + WS_QKVM); p.part = (float*)(ws + WS_PART);
  p.acc = (float*)(ws + WS_ACC); p.ctl = (unsigned*)(ws + WS_TMO); p.out = (u16*)d_out;

  (void)hipFuncSetAttribute((const void*)kv_kernel, hipFuncAttributeMaxDynamicSharedMemorySize, 2 * (256 * BK * 2 + B_BYTES));
  (void)hipFuncSetAttribute((const void*)mega_kernel, hipFuncAttributeMaxDynamicSharedMemorySize, MEGA_LDS);

  (void)hipMemsetAsync(ws + WS_TMO, 0, WS_ZERO_BYTES, stream);
  ln_kernel<<<S_ / 4, 256, 0, stream>>>(p.x, mem0, p.xn, p.memx, p.qkvm, p.part, p.acc);
  mega_kernel<<<256, 768, MEGA_LDS, stream>>>(p);
}
```

```cpp
#include <hip/hip_runtime.h>
#include <hip/hip_bf16.h>

typedef unsigned short u16;
typedef unsigned long long u64;
using bf16x8 = __attribute__((ext_vector_type(8))) short;
using bf16x4 = __attribute__((ext_vector_type(4))) short;
using f32x4 = __attribute__((ext_vector_type(4))) float;
using u32x4 = __attribute__((ext_vector_type(4))) unsigned;
typedef __attribute__((address_space(3))) bf16x4 lds_b4;
typedef __attribute__((address_space(1))) unsigned long long gu64;
typedef __attribute__((address_space(1))) unsigned gu32;

constexpr int S_ = 2048, DM = 1024, NH = 32, DH = 128, MM = 8, SEG = 32, NSEG = 64, FF = 3584;
constexpr int NQKV = 12288;
constexpr float SCALE = 0.08838834764831845f;

constexpr size_t WS_TMO = 0;
constexpr size_t WS_ZERO_BYTES = 32768;
constexpr int C_TMO = 0, C_TICKET = 32, C_Q = 64, C_H = 64 + 8 * 32, C_A = 64 + 16 * 32, C_KV = 64 + 48 * 32, C_F = 2048;
constexpr size_t WS_PART = 1ull << 20;
constexpr size_t WS_PART_BYTES = 4ull * 4 * 32 * 32 * 64 * 4;
constexpr size_t WS_XN = 8ull << 20;
constexpr size_t WS_QKV = 16ull << 20;
constexpr size_t WS_H = 72ull << 20;
constexpr size_t WS_O = 96ull << 20;
constexpr size_t WS_MEM = 120ull << 20;
constexpr size_t WS_QKVM = 122ull << 20;
constexpr size_t WS_ACC = 140ull << 20;

__device__ __forceinline__ float bf2f(u16 v) { return __uint_as_float(((unsigned)v) << 16); }
__device__ __forceinline__ u16 f2bf(float f) {
  __hip_bfloat16 h = __float2bfloat16(f);
  return *reinterpret_cast<u16*>(&h);
}
__device__ __forceinline__ float rbf(float f) { return bf2f(f2bf(f)); }
__device__ __forceinline__ unsigned pack2(float a, float b) {
  unsigned r = (unsigned)f2bf(a) | ((unsigned)f2bf(b) << 16);
  return r == 0xFFFFFFFFu ? 0x7FFF7FFFu : r;
}

__global__ __launch_bounds__(256) void ln_kernel(const u16* __restrict__ x, const u16* __restrict__ mem0,
                                                  u16* __restrict__ xn, u16* memx, u16* qkvm, float* part, float* acc) {
  int wave = threadIdx.x >> 6, lane = threadIdx.x & 63;
  int row = blockIdx.x * 4 + wave;
  const u16* xr = x + (size_t)row * DM;
  float v[16];
  bf16x8 a = *reinterpret_cast<const bf16x8*>(xr + lane * 8);
  bf16x8 b = *reinterpret_cast<const bf16x8*>(xr + 512 + lane * 8);
  float s = 0.f;
  for (int i = 0; i < 8; i++) { v[i] = bf2f((u16)a[i]); v[8 + i] = bf2f((u16)b[i]); s += v[i] + v[8 + i]; }
  for (int o = 32; o; o >>= 1) s += __shfl_xor(s, o);
  float m = s * (1.f / DM);
  float q = 0.f;
  for (int i = 0; i < 16; i++) { float d = v[i] - m; q += d * d; }
  for (int o = 32; o; o >>= 1) q += __shfl_xor(q, o);
  float r = rsqrtf(q * (1.f / DM) + 1e-5f);
  bf16x8 oa, ob;
  for (int i = 0; i < 8; i++) { oa[i] = (short)f2bf((v[i] - m) * r); ob[i] = (short)f2bf((v[8 + i] - m) * r); }
  *reinterpret_cast<bf16x8*>(xn + (size_t)row * DM + lane * 8) = oa;
  *reinterpret_cast<bf16x8*>(xn + (size_t)row * DM + 512 + lane * 8) = ob;
  size_t tid = (size_t)blockIdx.x * 256 + threadIdx.x, nth = (size_t)gridDim.x * 256;
  u32x4 P = {0xFFFFFFFFu, 0xFFFFFFFFu, 0xFFFFFFFFu, 0xFFFFFFFFu};
  for (size_t i = tid; i < (size_t)NSEG * MM * DM / 8; i += nth) {
    u32x4 val = P;
    if (i < MM * DM / 8) { int blk = (int)i >> 3, rw = (int)i & 7; val = reinterpret_cast<const u32x4*>(mem0)[rw * 128 + blk]; }
    reinterpret_cast<u32x4*>(memx)[i] = val;
  }
  for (size_t i = tid; i < (size_t)NSEG * MM * NQKV / 8; i += nth) reinterpret_cast<u32x4*>(qkvm)[i] = P;
  for (size_t i = tid; i < WS_PART_BYTES / 16; i += nth) reinterpret_cast<u32x4*>(part)[i] = P;
}

constexpr int BN = 128, BK = 64;
constexpr int B_BYTES = BK * BN * 2;

__device__ __forceinline__ float gelu_tanh(float x) {
  float u = 0.7978845608028654f * (x + 0.044715f * x * x * x);
  float t = 1.f - 2.f / (__expf(2.f * u) + 1.f);
  return 0.5f * x * (1.f + t);
}
__device__ __forceinline__ void st8_sc1(void* p, bf16x4 v) {
  asm volatile("global_store_dwordx2 %0, %1, off sc1\n\ts_nop 1" ::"v"(p), "v"(v) : "memory");
}

template <int BMT>
__device__ __forceinline__ void gemm_tile(const int EPI, char* smem, const u16* __restrict__ A, int lda, const u16* __restrict__ B, int ldb,
                                          int m0, int bcol, int k0, int nkt, void* out, int ldo, int ocol, const u16* bias) {
  constexpr int A_BYTES = BMT * BK * 2, STAGE = A_BYTES + B_BYTES;
  constexpr int WN = BMT == 256 ? 2 : 4, NT = BMT == 256 ? 4 : 2;
  int tid = threadIdx.x;
  asm volatile("" : "+v"(tid));
  const int wid = __builtin_amdgcn_readfirstlane(tid >> 6), lane = tid & 63, fr = lane & 15, fq = lane >> 4;
  const int wr = wid / WN, wc = wid % WN;
  auto stage = [&](int buf, int kt) {
    char* sa = smem + buf * STAGE;
    char* sb = sa + A_BYTES;
    int kk = k0 + kt * BK;
#pragma unroll
    for (int i = 0; i < BMT / 64; i++) {
      int pb = (wid * (BMT / 64) + i) * 1024 + lane * 16;
      int row = pb >> 7, chs = (pb >> 4) & 7, ch = chs ^ ((row >> 1) & 7);
      __builtin_amdgcn_global_load_lds((const unsigned*)(A + (size_t)(m0 + row) * lda + kk + ch * 8),
                                       (unsigned*)(sa + (wid * (BMT / 64) + i) * 1024), 16, 0, 0);
    }
#pragma unroll
    for (int i = 0; i < 2; i++) {
      int pb = (wid * 2 + i) * 1024 + lane * 16;
      int row = pb >> 8, chs = (pb >> 4) & 15, ch = chs ^ (((row & 3) << 2) | ((row >> 2) & 3));
      __builtin_amdgcn_global_load_lds((const unsigned*)(B + (size_t)(kk + row) * ldb + bcol + ch * 8),
                                       (unsigned*)(sb + (wid * 2 + i) * 1024), 16, 0, 0);
    }
  };
  f32x4 acc[4][NT];
#pragma unroll
  for (int i = 0; i < 4; i++)
#pragma unroll
    for (int j = 0; j < NT; j++) acc[i][j] = f32x4{0.f, 0.f, 0.f, 0.f};
  __syncthreads();
  stage(0, 0);
  stage(1, 1);
  int cur = 0;
  for (int t = 0; t < nkt; t++) {
    if (t + 1 < nkt) { if (BMT == 256) asm volatile("s_waitcnt vmcnt(6) lgkmcnt(0)" ::: "memory"); else asm volatile("s_waitcnt vmcnt(4) lgkmcnt(0)" ::: "memory"); }
    else asm volatile("s_waitcnt vmcnt(0) lgkmcnt(0)" ::: "memory");
    __builtin_amdgcn_s_barrier();
    asm volatile("" ::: "memory");
    int nxt = cur == 0 ? 2 : cur - 1;
    if (t + 2 < nkt) stage(nxt, t + 2);
    char* sa = smem + cur * STAGE;
    char* sb = sa + A_BYTES;
    cur = cur == 2 ? 0 : cur + 1;
#pragma unroll
    for (int ks = 0; ks < 2; ks++) {
      bf16x8 af[4], bfr[NT];
#pragma unroll
      for (int m = 0; m < 4; m++) {
        int row = wr * 64 + m * 16 + fr, ch = ks * 4 + fq;
        af[m] = *reinterpret_cast<const bf16x8*>(sa + row * 128 + 16 * (ch ^ ((row >> 1) & 7)));
      }
#pragma unroll
      for (int n = 0; n < NT; n++) {
        int c = wc * NT + n;
        int q = fr >> 2, pp = fr & 3;
        bf16x4 lo, hi;
        {
          int row = ks * 32 + 8 * fq + q, ch = 2 * c + (pp >> 1);
          int off = 256 * row + 16 * (ch ^ (((row & 3) << 2) | ((row >> 2) & 3))) + 8 * (pp & 1);
          lo = __builtin_amdgcn_ds_read_tr16_b64_v4i16((lds_b4*)(sb + off));
        }
        {
          int row = ks * 32 + 8 * fq + 4 + q, ch = 2 * c + (pp >> 1);
          int off = 256 * row + 16 * (ch ^ (((row & 3) << 2) | ((row >> 2) & 3))) + 8 * (pp & 1);
          hi = __builtin_amdgcn_ds_read_tr16_b64_v4i16((lds_b4*)(sb + off));
        }
        bfr[n] = bf16x8{lo[0], lo[1], lo[2], lo[3], hi[0], hi[1], hi[2], hi[3]};
      }
#pragma unroll
      for (int m = 0; m < 4; m++)
#pragma unroll
        for (int n = 0; n < NT; n++)
          acc[m][n] = __builtin_amdgcn_mfma_f32_16x16x32_bf16(bfr[n], af[m], acc[m][n], 0, 0, 0);
    }
  }
#pragma unroll
  for (int m = 0; m < 4; m++) {
    int row = m0 + wr * 64 + m * 16 + fr;
#pragma unroll
    for (int n = 0; n < NT; n++) {
      int col = ocol + (wc * NT + n) * 16 + fq * 4;
      if (EPI == 0 || EPI == 1) {
        bf16x4 o;
#pragma unroll
        for (int r = 0; r < 4; r++) o[r] = (short)f2bf(acc[m][n][r]);
        u16* dst = (u16*)out + (size_t)row * ldo + col;
        if (EPI == 0) *reinterpret_cast<bf16x4*>(dst) = o; else st8_sc1(dst, o);
      } else if (EPI == 2) {
        bf16x4 o, bb = *reinterpret_cast<const bf16x4*>(bias + col);
#pragma unroll
        for (int r = 0; r < 4; r++) o[r] = (short)f2bf(gelu_tanh(rbf(rbf(acc[m][n][r]) + bf2f((u16)bb[r]))));
        st8_sc1((u16*)out + (size_t)row * ldo + col, o);
      } else {
        float* dst = (float*)out + (size_t)row * ldo + col;
        asm volatile("global_store_dwordx4 %0, %1, off sc1\n\ts_nop 1" ::"v"(dst), "v"(acc[m][n]) : "memory");
      }
    }
  }
}

__global__ __launch_bounds__(512, 2) void kv_kernel(const u16* __restrict__ xn, const u16* __restrict__ Wk, const u16* __restrict__ Wv, u16* qkv) {
  extern __shared__ __attribute__((aligned(16))) char smem[];
  int b = blockIdx.x, m0 = (b & 7) * 256, nt = b >> 3;
  const u16* W = nt < 32 ? Wk : Wv;
  gemm_tile<256>(0, smem, xn, DM, W, 4096, m0, (nt & 31) * 128, 0, DM / BK, qkv, NQKV, 4096 + nt * 128, nullptr);
}

constexpr int KROW = 272;
constexpr int R_RED = 0;
constexpr int R_KT = 24576;
constexpr int R_VT = R_KT + 32 * KROW;
constexpr int R_RED2 = R_VT + 32 * KROW;
constexpr int R_X = R_RED2 + 2048;
constexpr int R_STG = R_X + 6144;
constexpr int R_WO = R_STG;
constexpr int R_LDS = R_STG + 12 * 8192;
#define RED_DELAY 8
#define DLY_B 0
#define DLY_A 0
#define STAMP(i)
#define ST2(i)

__device__ __forceinline__ float xor16(float x) {
  unsigned u = __float_as_uint(x);
  auto r = __builtin_amdgcn_permlane16_swap(u, u, false, false);
  unsigned a = r[0], b = r[1];
  return __uint_as_float(((threadIdx.x >> 4) & 1) ? a : b);
}
__device__ __forceinline__ float xor32(float x) {
  unsigned u = __float_as_uint(x);
  auto r = __builtin_amdgcn_permlane32_swap(u, u, false, false);
  unsigned a = r[0], b = r[1];
  return __uint_as_float(((threadIdx.x >> 5) & 1) ? a : b);
}
__device__ __forceinline__ bool poisoned(const u32x4& v) {
  return (v[0] == 0xFFFFFFFFu) | (v[1] == 0xFFFFFFFFu) | (v[2] == 0xFFFFFFFFu) | (v[3] == 0xFFFFFFFFu);
}
using u32x2 = __attribute__((ext_vector_type(2))) unsigned;

__device__ __forceinline__ void rec_role(char* smem, const u16* __restrict__ Wq, const u16* __restrict__ Wk,
                                         const u16* __restrict__ Wv, const u16* __restrict__ Wo,
                                         const u16* __restrict__ qkvt, u16* memx, u16* qkvm,
                                         float* part, unsigned* tmo) {
  const int tid = threadIdx.x, w = __builtin_amdgcn_readfirstlane(tid >> 6), lane = tid & 63, fr = lane & 15, g = lane >> 4;
  const int rb = blockIdx.x;
  const int h = rb >> 2, j = rb & 3;
  const unsigned spin_cap = 4000000;
  char* stg = smem + R_STG + w * 8192;
  if (tid < 8) *reinterpret_cast<volatile unsigned*>(smem + R_RED2 + 1024 + tid * 4) = 0u;
  auto stage_tokens = [&](int sn) {
    if ((sn & 7) == 0) {
      unsigned spins = 0;
      while (__hip_atomic_load((gu32*)(tmo + C_KV + (sn >> 3) * 32), __ATOMIC_RELAXED, __HIP_MEMORY_SCOPE_AGENT) < (sn == 0 ? 128u : 64u)) {
        __builtin_amdgcn_s_sleep(8);
        if (++spins > 40000000u) { __hip_atomic_store((gu32*)tmo, 7u, __ATOMIC_RELAXED, __HIP_MEMORY_SCOPE_AGENT); break; }
      }
    }
    int t0 = tid - 640;
    asm volatile("" : "+v"(t0));
#pragma unroll
    for (int i = 0; i < 8; i += 2) {
      int c = t0 + 128 * i, c1 = c + 128;
      const u16* s0 = qkvt + (size_t)(sn * SEG + ((c >> 4) & 31)) * NQKV + 4096 * (1 + (c >> 9)) + h * 128 + (c & 15) * 8;
      const u16* s1 = qkvt + (size_t)(sn * SEG + ((c1 >> 4) & 31)) * NQKV + 4096 * (1 + (c1 >> 9)) + h * 128 + (c1 & 15) * 8;
      u32x4 va, vb;
      asm volatile("global_load_dwordx4 %0, %2, off sc1\n\tglobal_load_dwordx4 %1, %3, off sc1\n\ts_waitcnt vmcnt(0)"
                   : "=&v"(va), "=&v"(vb) : "v"(s0), "v"(s1) : "memory");
      *reinterpret_cast<u32x4*>(smem + ((c >> 9) ? R_VT : R_KT) + ((c >> 4) & 31) * KROW + (c & 15) * 16) = va;
      *reinterpret_cast<u32x4*>(smem + ((c1 >> 9) ? R_VT : R_KT) + ((c1 >> 4) & 31) * KROW + (c1 & 15) * 16) = vb;
    }
  };

  if (w < 4) {
    __builtin_amdgcn_s_setprio(3);
    bf16x8 wo[4][4];
#pragma unroll
    for (int half = 0; half < 2; half++) {
#pragma unroll
      for (int i = 0; i < 8; i++) {
        int c = i * 64 + lane, row = c >> 2, ch = c & 3;
        u32x4 v = *reinterpret_cast<const u32x4*>(Wo + (size_t)(h * 128 + row) * DM + 256 * j + 64 * w + 32 * half + ch * 8);
        *reinterpret_cast<u32x4*>(stg + row * 64 + ch * 16) = v;
      }
      __builtin_amdgcn_s_waitcnt(0);
      __builtin_amdgcn_wave_barrier();
#pragma unroll
      for (int t2 = 0; t2 < 2; t2++)
#pragma unroll
        for (int kk = 0; kk < 4; kk++) {
          int q = fr >> 2, pp = fr & 3;
          bf16x4 lo = __builtin_amdgcn_ds_read_tr16_b64_v4i16((lds_b4*)(stg + (kk * 32 + 4 * g + q) * 64 + t2 * 32 + pp * 8));
          bf16x4 hi = __builtin_amdgcn_ds_read_tr16_b64_v4i16((lds_b4*)(stg + (kk * 32 + 16 + 4 * g + q) * 64 + t2 * 32 + pp * 8));
          wo[half * 2 + t2][kk] = bf16x8{lo[0], lo[1], lo[2], lo[3], hi[0], hi[1], hi[2], hi[3]};
        }
      __builtin_amdgcn_s_waitcnt(0);
      __builtin_amdgcn_wave_barrier();
    }
    __syncthreads();
#pragma unroll
    for (int t4 = 0; t4 < 4; t4++)
#pragma unroll
      for (int kk = 0; kk < 4; kk++) *reinterpret_cast<bf16x8*>(smem + R_WO + w * 16384 + ((t4 * 4 + kk) * 64 + lane) * 16) = wo[t4][kk];
    for (int s = 0; s < NSEG; s++) {
      __syncthreads();
      bf16x8 kt[2][4], vt[8];
      {
        const char* kb = smem + R_KT;
        const char* vb = smem + R_VT;
#pragma unroll
        for (int t = 0; t < 2; t++)
#pragma unroll
          for (int ks = 0; ks < 4; ks++) kt[t][ks] = *reinterpret_cast<const bf16x8*>(kb + (16 * t + fr) * KROW + ks * 64 + g * 16);
        int q = fr >> 2, pp = fr & 3;
#pragma unroll
        for (int et = 0; et < 8; et++) {
          bf16x4 lo = __builtin_amdgcn_ds_read_tr16_b64_v4i16((lds_b4*)(vb + (4 * g + q) * KROW + et * 32 + pp * 8));
          bf16x4 hi = __builtin_amdgcn_ds_read_tr16_b64_v4i16((lds_b4*)(vb + (16 + 4 * g + q) * KROW + et * 32 + pp * 8));
          vt[et] = bf16x8{lo[0], lo[1], lo[2], lo[3], hi[0], hi[1], hi[2], hi[3]};
        }
      }
      STAMP(3)
      __syncthreads();
      u32x4 q0, q1, q2, q3, k0, k1, k2, k3;
      u32x2 v0, v1, v2, v3, v4, v5, v6, v7;
      {
        const char* qp = smem + R_X + ((g >> 1) * 128 + (fr & 7) * 16 + (g & 1) * 8) * 2;
        const char* vp = smem + R_X + (512 + fr * 8 + (g & 1) * 4) * 2;
        q0 = *reinterpret_cast<const u32x4*>(qp); q1 = *reinterpret_cast<const u32x4*>(qp + 1536);
        q2 = *reinterpret_cast<const u32x4*>(qp + 3072); q3 = *reinterpret_cast<const u32x4*>(qp + 4608);
        k0 = *reinterpret_cast<const u32x4*>(qp + 512); k1 = *reinterpret_cast<const u32x4*>(qp + 2048);
        k2 = *reinterpret_cast<const u32x4*>(qp + 3584); k3 = *reinterpret_cast<const u32x4*>(qp + 5120);
        v0 = *reinterpret_cast<const u32x2*>(vp); v1 = *reinterpret_cast<const u32x2*>(vp + 256);
        v2 = *reinterpret_cast<const u32x2*>(vp + 1536); v3 = *reinterpret_cast<const u32x2*>(vp + 1792);
        v4 = *reinterpret_cast<const u32x2*>(vp + 3072); v5 = *reinterpret_cast<const u32x2*>(vp + 3328);
        v6 = *reinterpret_cast<const u32x2*>(vp + 4608); v7 = *reinterpret_cast<const u32x2*>(vp + 4864);
        asm volatile("" : "+v"(q0), "+v"(q1), "+v"(q2), "+v"(q3), "+v"(k0), "+v"(k1), "+v"(k2), "+v"(k3),
                     "+v"(v0), "+v"(v1), "+v"(v2), "+v"(v3), "+v"(v4), "+v"(v5), "+v"(v6), "+v"(v7));
      }
      STAMP(4)
      f32x4 st[3];
      {
        bf16x8 qf[4] = {__builtin_bit_cast(bf16x8, q0), __builtin_bit_cast(bf16x8, q1), __builtin_bit_cast(bf16x8, q2), __builtin_bit_cast(bf16x8, q3)};
        bf16x8 km[4] = {__builtin_bit_cast(bf16x8, k0), __builtin_bit_cast(bf16x8, k1), __builtin_bit_cast(bf16x8, k2), __builtin_bit_cast(bf16x8, k3)};
#pragma unroll
        for (int t = 0; t < 3; t++) st[t] = f32x4{0.f, 0.f, 0.f, 0.f};
#pragma unroll
        for (int ks = 0; ks < 4; ks++) {
          st[0] = __builtin_amdgcn_mfma_f32_16x16x32_bf16(kt[0][ks], qf[ks], st[0], 0, 0, 0);
          st[1] = __builtin_amdgcn_mfma_f32_16x16x32_bf16(kt[1][ks], qf[ks], st[1], 0, 0, 0);
          st[2] = __builtin_amdgcn_mfma_f32_16x16x32_bf16(km[ks], qf[ks], st[2], 0, 0, 0);
        }
      }
      bf16x8 wf[4][4];
      asm volatile("" ::: "memory");
#pragma unroll
      for (int t4 = 0; t4 < 4; t4++)
#pragma unroll
        for (int kk = 0; kk < 4; kk++) wf[t4][kk] = *reinterpret_cast<const bf16x8*>(smem + R_WO + w * 16384 + ((t4 * 4 + kk) * 64 + lane) * 16);
      asm volatile("" ::: "memory");
      if (g >= 2) st[2] = f32x4{-3.0e38f, -3.0e38f, -3.0e38f, -3.0e38f};
      float mx = fmaxf(fmaxf(fmaxf(st[0][0], st[0][1]), fmaxf(st[0][2], st[0][3])), fmaxf(fmaxf(st[1][0], st[1][1]), fmaxf(st[1][2], st[1][3])));
      mx = fmaxf(mx, fmaxf(fmaxf(st[2][0], st[2][1]), fmaxf(st[2][2], st[2][3])));
      mx = fmaxf(mx, xor16(mx));
      mx = fmaxf(mx, xor32(mx));
      const float c2 = SCALE * 1.4426950408889634f;
      float mc = mx * c2;
      float sum = 0.f;
#pragma unroll
      for (int t = 0; t < 3; t++)
#pragma unroll
        for (int r = 0; r < 4; r++) {
          float e = __builtin_amdgcn_exp2f(__builtin_fmaf(st[t][r], c2, -mc));
          st[t][r] = e;
          sum += e;
        }
      sum += xor16(sum);
      sum += xor32(sum);
      float inv = __builtin_amdgcn_rcpf(sum);
      bf16x8 pf[2];
#pragma unroll
      for (int r = 0; r < 4; r++) {
        pf[0][r] = (short)f2bf(st[0][r] * inv);
        pf[0][4 + r] = (short)f2bf(st[1][r] * inv);
        pf[1][r] = (short)f2bf(st[2][r] * inv);
        pf[1][4 + r] = 0;
      }
      bf16x8 of[4];
      {
        u32x2 vv[8] = {v0, v1, v2, v3, v4, v5, v6, v7};
#pragma unroll
        for (int et = 0; et < 8; et++) {
          f32x4 o = {0.f, 0.f, 0.f, 0.f};
          o = __builtin_amdgcn_mfma_f32_16x16x32_bf16(vt[et], pf[0], o, 0, 0, 0);
          u32x4 vx = {vv[et][0], vv[et][1], 0u, 0u};
          o = __builtin_amdgcn_mfma_f32_16x16x32_bf16(__builtin_bit_cast(bf16x8, vx), pf[1], o, 0, 0, 0);
#pragma unroll
          for (int r = 0; r < 4; r++) of[et >> 1][(et & 1) * 4 + r] = (short)f2bf(o[r]);
        }
      }
      if (s + 1 < NSEG) {
        f32x4 pa[4];
#pragma unroll
        for (int t4 = 0; t4 < 4; t4++) pa[t4] = f32x4{0.f, 0.f, 0.f, 0.f};
#pragma unroll
        for (int kk = 0; kk < 4; kk++)
#pragma unroll
          for (int t4 = 0; t4 < 4; t4++) pa[t4] = __builtin_amdgcn_mfma_f32_16x16x32_bf16(of[kk], wf[t4][kk], pa[t4], 0, 0, 0);
        if (g < 2) {
#pragma unroll
          for (int t4 = 0; t4 < 4; t4++) {
            int col = 64 * w + 16 * t4 + fr;
            int dst = col >> 3, cc = col & 7;
            float* base = part + ((((size_t)(s & 3) * 4 + j) * 32 + dst) * 32 + h) * 64 + cc * 8 + 4 * g;
            u32x4 vv;
#pragma unroll
            for (int r = 0; r < 4; r++) { unsigned u = __float_as_uint(pa[t4][r]); vv[r] = u == 0xFFFFFFFFu ? 0x7FFFFFFFu : u; }
            asm volatile("global_store_dwordx4 %0, %1, off sc1\n\ts_nop 1" :: "v"(base), "v"(vv) : "memory");
          }
        }
      }
      STAMP(5)
    }
  } else {
    const int gw = w - 4;
    bf16x8 wb[6][4];
#pragma unroll
    for (int mat = 0; mat < 3; mat++) {
      const u16* W = mat == 0 ? Wq : (mat == 1 ? Wk : Wv);
#pragma unroll
      for (int i = 0; i < 8; i++) {
        int c = i * 64 + lane, row = c >> 2, ch = c & 3;
        u32x4 v = *reinterpret_cast<const u32x4*>(W + (size_t)(128 * gw + row) * 4096 + h * 128 + 32 * j + ch * 8);
        *reinterpret_cast<u32x4*>(stg + row * 64 + ch * 16) = v;
      }
      __builtin_amdgcn_s_waitcnt(0);
      __builtin_amdgcn_wave_barrier();
#pragma unroll
      for (int t2 = 0; t2 < 2; t2++)
#pragma unroll
        for (int ks = 0; ks < 4; ks++) {
          int q = fr >> 2, pp = fr & 3;
          bf16x4 lo = __builtin_amdgcn_ds_read_tr16_b64_v4i16((lds_b4*)(stg + (ks * 32 + 8 * g + q) * 64 + t2 * 32 + pp * 8));
          bf16x4 hi = __builtin_amdgcn_ds_read_tr16_b64_v4i16((lds_b4*)(stg + (ks * 32 + 8 * g + 4 + q) * 64 + t2 * 32 + pp * 8));
          wb[mat * 2 + t2][ks] = bf16x8{lo[0], lo[1], lo[2], lo[3], hi[0], hi[1], hi[2], hi[3]};
        }
      __builtin_amdgcn_s_waitcnt(0);
      __builtin_amdgcn_wave_barrier();
    }
    if (gw >= 6) stage_tokens(0);
    __syncthreads();
    for (int s = 0; s < NSEG; s++) {
      if (s > 0) { volatile unsigned* pflag = reinterpret_cast<volatile unsigned*>(smem + R_RED2 + 1028); while (*pflag != (unsigned)s) __builtin_amdgcn_s_sleep(2); }
      if (gw == 0) { ST2(0) }
      for (int i = 0; i < DLY_A; i++) __builtin_amdgcn_s_sleep(1);
      u32x4 a0, a1, a2, a3;
      {
        const u16* mp = memx + (size_t)s * MM * DM + (16 * gw + g) * 64 + (fr & 7) * 8;
        unsigned spins = 0;
        while (true) {
          asm volatile(
              "global_load_dwordx4 %0, %4, off sc1\n\t"
              "global_load_dwordx4 %1, %4, off offset:512 sc1\n\t"
              "global_load_dwordx4 %2, %4, off offset:1024 sc1\n\t"
              "global_load_dwordx4 %3, %4, off offset:1536 sc1\n\t"
              "s_waitcnt vmcnt(0)"
              : "=&v"(a0), "=&v"(a1), "=&v"(a2), "=&v"(a3) : "v"(mp) : "memory");
          bool bad = poisoned(a0) | poisoned(a1) | poisoned(a2) | poisoned(a3);
          if (!__any(bad)) break;
          if (++spins > spin_cap) { if (lane == 0) __hip_atomic_store((gu32*)tmo, 1u, __ATOMIC_RELAXED, __HIP_MEMORY_SCOPE_AGENT); break; }
        }
      }
      if (gw == 0) { ST2(1) }
      {
        bf16x8 af[4] = {__builtin_bit_cast(bf16x8, a0), __builtin_bit_cast(bf16x8, a1), __builtin_bit_cast(bf16x8, a2), __builtin_bit_cast(bf16x8, a3)};
        f32x4 acc[6];
#pragma unroll
        for (int t = 0; t < 6; t++) acc[t] = f32x4{0.f, 0.f, 0.f, 0.f};
#pragma unroll
        for (int ks = 0; ks < 4; ks++)
#pragma unroll
          for (int t = 0; t < 6; t++) acc[t] = __builtin_amdgcn_mfma_f32_16x16x32_bf16(af[ks], wb[t][ks], acc[t], 0, 0, 0);
        if (g < 2) {
          float* red = reinterpret_cast<float*>(smem + R_RED) + gw * 768;
#pragma unroll
          for (int t = 0; t < 4; t++)
#pragma unroll
            for (int r = 0; r < 4; r++) red[t * 128 + (4 * g + r) * 16 + fr] = acc[t][r];
#pragma unroll
          for (int t = 4; t < 6; t++) *reinterpret_cast<f32x4*>(red + t * 128 + fr * 8 + 4 * g) = acc[t];
        }
      }
      if (gw == 0) { ST2(2) }
      __syncthreads();
      if (gw < 2) {
        if (gw == 0 || lane < 32) {
          int off = gw * 512 + lane * 8;
          const float* red = reinterpret_cast<const float*>(smem + R_RED) + off;
          f32x4 x0 = {0.f, 0.f, 0.f, 0.f}, x1 = x0;
#pragma unroll
          for (int ww = 0; ww < 8; ww++) {
            x0 += *reinterpret_cast<const f32x4*>(red + ww * 768);
            x1 += *reinterpret_cast<const f32x4*>(red + ww * 768 + 4);
          }
          u32x4 v4 = {pack2(x0[0], x0[1]), pack2(x0[2], x0[3]), pack2(x1[0], x1[1]), pack2(x1[2], x1[3])};
          u16* dst = qkvm + (((size_t)s * NH + h) * 4 + j) * 768 + off;
          asm volatile("global_store_dwordx4 %0, %1, off sc1\n\ts_nop 1" ::"v"(dst), "v"(v4) : "memory");
        }
      } else {
        int c = tid - 384;
        const u16* src = qkvm + ((size_t)s * NH + h) * 3072 + c * 8;
        unsigned spins = 0;
        for (int i = 0; i < DLY_B; i++) __builtin_amdgcn_s_sleep(1);
        u32x4 v;
        while (true) {
          asm volatile("global_load_dwordx4 %0, %1, off sc1\n\ts_waitcnt vmcnt(0)" : "=&v"(v) : "v"(src) : "memory");
          if (!__any(poisoned(v))) break;
          if (++spins > spin_cap) { if (lane == 0) __hip_atomic_store((gu32*)tmo, 2u, __ATOMIC_RELAXED, __HIP_MEMORY_SCOPE_AGENT); break; }
        }
        *reinterpret_cast<u32x4*>(smem + R_X + c * 16) = v;
      }
      __syncthreads();
      if (gw < 2) {
      } else if (gw < 4) {
        if (s > 0) {
          int ln = lane; asm volatile("" : "+v"(ln));
          float* base = part + ((((size_t)((s - 1) & 3) * 4 + j) * 32 + h) * 32 + 16 * (gw - 2)) * 64 + ln * 4;
          u32x4 P = {0xFFFFFFFFu, 0xFFFFFFFFu, 0xFFFFFFFFu, 0xFFFFFFFFu};
          asm volatile(
              "global_store_dwordx4 %0, %1, off sc1\n\t"
              "global_store_dwordx4 %0, %1, off offset:1024 sc1\n\t"
              "global_store_dwordx4 %0, %1, off offset:2048 sc1\n\t"
              "global_store_dwordx4 %0, %1, off offset:3072 sc1\n\ts_nop 1" ::"v"(base), "v"(P) : "memory");
        }
      } else if (gw < 6) {
        if (s + 1 < NSEG) {
          int ln = lane; asm volatile("" : "+v"(ln));
          float* base = part + ((((size_t)(s & 3) * 4 + j) * 32 + h) * 32 + 16 * (gw - 4)) * 64 + ln * 4;
          unsigned spins = 0;
          u32x4 b0, b1, b2, b3;
          for (int i = 0; i < RED_DELAY; i++) __builtin_amdgcn_s_sleep(1);
          while (true) {
            asm volatile(
                "global_load_dwordx4 %0, %4, off sc1\n\t"
                "global_load_dwordx4 %1, %4, off offset:1024 sc1\n\t"
                "global_load_dwordx4 %2, %4, off offset:2048 sc1\n\t"
                "global_load_dwordx4 %3, %4, off offset:3072 sc1\n\t"
                "s_waitcnt vmcnt(0)"
                : "=&v"(b0), "=&v"(b1), "=&v"(b2), "=&v"(b3) : "v"(base) : "memory");
            bool bad = poisoned(b0) | poisoned(b1) | poisoned(b2) | poisoned(b3);
            if (!__any(bad)) break;
            if (++spins > spin_cap) { if (lane == 0) __hip_atomic_store((gu32*)tmo, 3u, __ATOMIC_RELAXED, __HIP_MEMORY_SCOPE_AGENT); break; }
          }
          f32x4 val = (__builtin_bit_cast(f32x4, b0) + __builtin_bit_cast(f32x4, b1)) + (__builtin_bit_cast(f32x4, b2) + __builtin_bit_cast(f32x4, b3));
#pragma unroll
          for (int i = 0; i < 4; i++) { val[i] += xor16(val[i]); val[i] += xor32(val[i]); }
          float* r2 = reinterpret_cast<float*>(smem + R_RED2) + (gw - 4) * 64;
          volatile unsigned* lflag = reinterpret_cast<volatile unsigned*>(smem + R_RED2 + 1024);
          if (lane < 16) {
#pragma unroll
            for (int i = 0; i < 4; i++) r2[(4 * (fr & 1) + i) * 8 + (fr >> 1)] = val[i];
          }
          __builtin_amdgcn_s_waitcnt(0xc07f);
          __builtin_amdgcn_wave_barrier();
          if (gw == 5) {
            if (lane == 0) *lflag = (unsigned)(s + 1);
          } else {
            while (*lflag != (unsigned)(s + 1)) {}
            if (lane < 8) {
              const float* q2 = reinterpret_cast<const float*>(smem + R_RED2) + lane * 8;
              f32x4 x0 = *reinterpret_cast<const f32x4*>(q2) + *reinterpret_cast<const f32x4*>(q2 + 64);
              f32x4 x1 = *reinterpret_cast<const f32x4*>(q2 + 4) + *reinterpret_cast<const f32x4*>(q2 + 68);
              u32x4 v4 = {pack2(x0[0], x0[1]), pack2(x0[2], x0[3]), pack2(x1[0], x1[1]), pack2(x1[2], x1[3])};
              u16* dst = memx + (size_t)(s + 1) * MM * DM + (32 * j + h) * 64 + ln * 8;
              asm volatile("global_store_dwordx4 %0, %1, off sc1\n\ts_nop 1" ::"v"(dst), "v"(v4) : "memory");
            }
            if (lane == 0) *reinterpret_cast<volatile unsigned*>(smem + R_RED2 + 1028) = (unsigned)(s + 1);
          }
        }
      } else {
        if (s + 1 < NSEG) stage_tokens(s + 1);
      }
    }
  }
}

struct MegaP {
  const u16* x; const u16* Wq; const u16* Wk; const u16* Wv; const u16* Wo; const u16* w1; const u16* b1; const u16* w2; const u16* b2;
  u16* xn; u16* qkv; u16* H; u16* O; u16* memx; u16* qkvm; float* part; float* acc; unsigned* ctl; u16* out;
};

__device__ __forceinline__ void wait_count(unsigned* word, unsigned target, unsigned* tmo) {
  if (threadIdx.x == 0) {
    unsigned spins = 0;
    while (__hip_atomic_load((gu32*)word, __ATOMIC_RELAXED, __HIP_MEMORY_SCOPE_AGENT) < target) {
      __builtin_amdgcn_s_sleep(8);
      if (++spins > 40000000u) { __hip_atomic_store((gu32*)tmo, 9u, __ATOMIC_RELAXED, __HIP_MEMORY_SCOPE_AGENT); break; }
    }
    __builtin_amdgcn_fence(__ATOMIC_ACQUIRE, "agent");
    asm volatile("s_waitcnt vmcnt(0)" ::: "memory");
  }
  __syncthreads();
}
__device__ __forceinline__ unsigned signal_count(unsigned* word, char* smem) {
  asm volatile("s_waitcnt vmcnt(0)" ::: "memory");
  __syncthreads();
  volatile unsigned* bc = reinterpret_cast<volatile unsigned*>(smem + 150 * 1024);
  if (threadIdx.x == 0) *bc = __hip_atomic_fetch_add((gu32*)word, 1u, __ATOMIC_RELAXED, __HIP_MEMORY_SCOPE_AGENT);
  __syncthreads();
  return *bc;
}

__device__ __forceinline__ void final_tile(const MegaP& p, int mt, int nt) {
  int tid = threadIdx.x;
  asm volatile("" : "+v"(tid));
  constexpr size_t SL = (size_t)S_ * DM;
#pragma unroll
  for (int i = 0; i < 8; i += 2) {
    size_t e0 = (size_t)(mt * 128 + i * 16 + (tid >> 5)) * DM + nt * 128 + (tid & 31) * 4, e1 = e0 + 16 * DM;
    f32x4 a[5], b[5];
    asm volatile("global_load_dwordx4 %0, %10, off sc1\n\tglobal_load_dwordx4 %1, %11, off sc1\n\tglobal_load_dwordx4 %2, %12, off sc1\n\t"
                 "global_load_dwordx4 %3, %13, off sc1\n\tglobal_load_dwordx4 %4, %14, off sc1\n\t"
                 "global_load_dwordx4 %5, %15, off sc1\n\tglobal_load_dwordx4 %6, %16, off sc1\n\tglobal_load_dwordx4 %7, %17, off sc1\n\t"
                 "global_load_dwordx4 %8, %18, off sc1\n\tglobal_load_dwordx4 %9, %19, off sc1\n\ts_waitcnt vmcnt(0)"
                 : "=&v"(a[0]), "=&v"(a[1]), "=&v"(a[2]), "=&v"(a[3]), "=&v"(a[4]), "=&v"(b[0]), "=&v"(b[1]), "=&v"(b[2]), "=&v"(b[3]), "=&v"(b[4])
                 : "v"(p.acc + e0), "v"(p.acc + SL + e0), "v"(p.acc + 2 * SL + e0), "v"(p.acc + 3 * SL + e0), "v"(p.acc + 4 * SL + e0),
                   "v"(p.acc + e1), "v"(p.acc + SL + e1), "v"(p.acc + 2 * SL + e1), "v"(p.acc + 3 * SL + e1), "v"(p.acc + 4 * SL + e1) : "memory");
    f32x4 sa = (a[0] + a[1]) + (a[2] + a[3]) + a[4], sb = (b[0] + b[1]) + (b[2] + b[3]) + b[4];
    bf16x4 bv = *reinterpret_cast<const bf16x4*>(p.b2 + (e0 & (DM - 1)));
    bf16x4 x0 = *reinterpret_cast<const bf16x4*>(p.x + e0), x1 = *reinterpret_cast<const bf16x4*>(p.x + e1);
    bf16x4 o0, o1;
#pragma unroll
    for (int r = 0; r < 4; r++) {
      o0[r] = (short)f2bf(bf2f((u16)x0[r]) + sa[r] + bf2f((u16)bv[r]));
      o1[r] = (short)f2bf(bf2f((u16)x1[r]) + sb[r] + bf2f((u16)bv[r]));
    }
    *reinterpret_cast<bf16x4*>(p.out + e0) = o0;
    *reinterpret_cast<bf16x4*>(p.out + e1) = o1;
  }
}

__device__ __forceinline__ void attn_task(char* smem, const MegaP& p, int s, int hg) {
  int tid = threadIdx.x;
  asm volatile("" : "+v"(tid));
  const int w = __builtin_amdgcn_readfirstlane(tid >> 6), lane = tid & 63, fr = lane & 15, g = lane >> 4;
  const int h = hg * 8 + w;
  char* sV = smem + w * 32 * KROW;
  const u16* tok = p.qkv + (size_t)s * SEG * NQKV + h * 128;
#pragma unroll
  for (int i = 0; i < 8; i++) {
    int c = i * 64 + lane, row = c >> 4, ch = c & 15;
    *reinterpret_cast<u32x4*>(sV + row * KROW + ch * 16) = *reinterpret_cast<const u32x4*>(tok + (size_t)row * NQKV + 8192 + ch * 8);
  }
  bf16x8 kt[2][4];
#pragma unroll
  for (int t = 0; t < 2; t++)
#pragma unroll
    for (int ks = 0; ks < 4; ks++) kt[t][ks] = *reinterpret_cast<const bf16x8*>(tok + (size_t)(16 * t + fr) * NQKV + 4096 + ks * 32 + g * 8);
  u32x4 k0, k1, k2, k3;
  u32x2 v0, v1, v2, v3, v4, v5, v6, v7;
  {
    const u16* eb = p.qkvm + ((size_t)s * NH + h) * 3072;
    const u16* qp = eb + (g >> 1) * 128 + (fr & 7) * 16 + (g & 1) * 8;
    const u16* vp = eb + 512 + fr * 8 + (g & 1) * 4;
    unsigned spins = 0;
    while (true) {
      asm volatile(
          "global_load_dwordx4 %0, %12, off offset:512 sc1\n\t"
          "global_load_dwordx4 %1, %12, off offset:2048 sc1\n\t"
          "global_load_dwordx4 %2, %12, off offset:3584 sc1\n\t"
          "global_load_dwordx4 %3, %13, off offset:1024 sc1\n\t"
          "global_load_dwordx2 %4, %14, off sc1\n\t"
          "global_load_dwordx2 %5, %14, off offset:256 sc1\n\t"
          "global_load_dwordx2 %6, %14, off offset:1536 sc1\n\t"
          "global_load_dwordx2 %7, %14, off offset:1792 sc1\n\t"
          "global_load_dwordx2 %8, %14, off offset:3072 sc1\n\t"
          "global_load_dwordx2 %9, %14, off offset:3328 sc1\n\t"
          "global_load_dwordx2 %10, %15, off offset:512 sc1\n\t"
          "global_load_dwordx2 %11, %15, off offset:768 sc1\n\t"
          "s_waitcnt vmcnt(0)"
          : "=&v"(k0), "=&v"(k1), "=&v"(k2), "=&v"(k3), "=&v"(v0), "=&v"(v1), "=&v"(v2), "=&v"(v3), "=&v"(v4), "=&v"(v5), "=&v"(v6), "=&v"(v7)
          : "v"(qp), "v"(qp + 2048), "v"(vp), "v"(vp + 2048) : "memory");
      bool bad = poisoned(k0) | poisoned(k1) | poisoned(k2) | poisoned(k3);
      bad |= (v0[0] == 0xFFFFFFFFu) | (v0[1] == 0xFFFFFFFFu) | (v1[0] == 0xFFFFFFFFu) | (v1[1] == 0xFFFFFFFFu) |
             (v2[0] == 0xFFFFFFFFu) | (v2[1] == 0xFFFFFFFFu) | (v3[0] == 0xFFFFFFFFu) | (v3[1] == 0xFFFFFFFFu) |
             (v4[0] == 0xFFFFFFFFu) | (v4[1] == 0xFFFFFFFFu) | (v5[0] == 0xFFFFFFFFu) | (v5[1] == 0xFFFFFFFFu) |
             (v6[0] == 0xFFFFFFFFu) | (v6[1] == 0xFFFFFFFFu) | (v7[0] == 0xFFFFFFFFu) | (v7[1] == 0xFFFFFFFFu);
      if (!__any(bad)) break;
      __builtin_amdgcn_s_sleep(32);
      if (++spins > 4000000u) { if (lane == 0) __hip_atomic_store((gu32*)p.ctl, 5u, __ATOMIC_RELAXED, __HIP_MEMORY_SCOPE_AGENT); break; }
    }
  }
  bf16x8 km[4] = {__builtin_bit_cast(bf16x8, k0), __builtin_bit_cast(bf16x8, k1), __builtin_bit_cast(bf16x8, k2), __builtin_bit_cast(bf16x8, k3)};
  u32x2 vv[8] = {v0, v1, v2, v3, v4, v5, v6, v7};
  __builtin_amdgcn_s_waitcnt(0);
  __builtin_amdgcn_wave_barrier();
#pragma unroll
  for (int qt = 0; qt < 2; qt++) {
    const int qi = qt * 16 + fr;
    f32x4 st[3];
#pragma unroll
    for (int t = 0; t < 3; t++) st[t] = f32x4{0.f, 0.f, 0.f, 0.f};
#pragma unroll
    for (int ks = 0; ks < 4; ks++) {
      bf16x8 qf = *reinterpret_cast<const bf16x8*>(tok + (size_t)qi * NQKV + ks * 32 + g * 8);
      st[0] = __builtin_amdgcn_mfma_f32_16x16x32_bf16(kt[0][ks], qf, st[0], 0, 0, 0);
      st[1] = __builtin_amdgcn_mfma_f32_16x16x32_bf16(kt[1][ks], qf, st[1], 0, 0, 0);
      st[2] = __builtin_amdgcn_mfma_f32_16x16x32_bf16(km[ks], qf, st[2], 0, 0, 0);
    }
    float mx = -3.0e38f;
#pragma unroll
    for (int t = 0; t < 3; t++)
#pragma unroll
      for (int r = 0; r < 4; r++) {
        bool masked = t < 2 ? (16 * t + 4 * g + r > qi) : (g >= 2);
        float v = masked ? -3.0e38f : st[t][r];
        st[t][r] = v;
        mx = fmaxf(mx, v);
      }
    mx = fmaxf(mx, xor16(mx));
    mx = fmaxf(mx, xor32(mx));
    const float c2 = SCALE * 1.4426950408889634f;
    float mc = mx * c2, sum = 0.f;
#pragma unroll
    for (int t = 0; t < 3; t++)
#pragma unroll
      for (int r = 0; r < 4; r++) {
        float e = __builtin_amdgcn_exp2f(__builtin_fmaf(st[t][r], c2, -mc));
        st[t][r] = e;
        sum += e;
      }
    sum += xor16(sum);
    sum += xor32(sum);
    float inv = __builtin_amdgcn_rcpf(sum);
    bf16x8 pf[2];
#pragma unroll
    for (int r = 0; r < 4; r++) {
      pf[0][r] = (short)f2bf(st[0][r] * inv);
      pf[0][4 + r] = (short)f2bf(st[1][r] * inv);
      pf[1][r] = (short)f2bf(st[2][r] * inv);
      pf[1][4 + r] = 0;
    }
    int q = fr >> 2, pp = fr & 3;
    u16* op = p.O + (size_t)(s * SEG + qi) * 4096 + h * 128;
#pragma unroll
    for (int et = 0; et < 8; et++) {
      bf16x4 lo = __builtin_amdgcn_ds_read_tr16_b64_v4i16((lds_b4*)(sV + (4 * g + q) * KROW + et * 32 + pp * 8));
      bf16x4 hi = __builtin_amdgcn_ds_read_tr16_b64_v4i16((lds_b4*)(sV + (16 + 4 * g + q) * KROW + et * 32 + pp * 8));
      bf16x8 vf = bf16x8{lo[0], lo[1], lo[2], lo[3], hi[0], hi[1], hi[2], hi[3]};
      f32x4 o = {0.f, 0.f, 0.f, 0.f};
      o = __builtin_amdgcn_mfma_f32_16x16x32_bf16(vf, pf[0], o, 0, 0, 0);
      u32x4 vx = {vv[et][0], vv[et][1], 0u, 0u};
      o = __builtin_amdgcn_mfma_f32_16x16x32_bf16(__builtin_bit_cast(bf16x8, vx), pf[1], o, 0, 0, 0);
      bf16x4 ov;
#pragma unroll
      for (int r = 0; r < 4; r++) ov[r] = (short)f2bf(o[r]);
      st8_sc1(op + et * 16 + g * 4, ov);
    }
  }
}

constexpr int T_K0 = 128, T_KV = T_K0 + 448, T_Q = T_KV + 256, T_H = T_Q + 224, T_B = T_H + 64, T_END = T_B + 16 * 48;
constexpr int MEGA_LDS = 152 * 1024;

__global__ __launch_bounds__(768, 3) void mega_kernel(MegaP p) {
  extern __shared__ __attribute__((aligned(16))) char smem[];
  if (blockIdx.x < 128) {
    rec_role(smem, p.Wq, p.Wk, p.Wv, p.Wo, p.qkv, p.memx, p.qkvm, p.part, p.ctl);
    return;
  }
  if (threadIdx.x >= 512) return;
  volatile unsigned* bc = reinterpret_cast<volatile unsigned*>(smem + 150 * 1024 + 16);
  while (true) {
    __syncthreads();
    if (threadIdx.x == 0) *bc = __hip_atomic_fetch_add((gu32*)(p.ctl + C_TICKET), 1u, __ATOMIC_RELAXED, __HIP_MEMORY_SCOPE_AGENT);
    __syncthreads();
    int t = (int)*bc;
    if (t >= T_END) break;
    if (t < T_K0) {
      int half = t >> 6, n2 = t & 63;
      gemm_tile<128>(1, smem, p.xn, DM, n2 < 32 ? p.Wk : p.Wv, 4096, half * 128, (n2 & 31) * 128, 0, DM / BK, p.qkv, NQKV, 4096 + n2 * 128, nullptr);
      signal_count(p.ctl + C_KV, smem);
    } else if (t < T_B) {
      int epi, m, nt, k0, nkt, lda, ldb, ldo; const u16* A; const u16* B; void* out; unsigned* sig;
      int ocol_add = 0;
      if (t < T_KV) {
        m = 1 + ((t - T_K0) >> 6); int n2 = (t - T_K0) & 63; nt = n2 & 31; epi = 1; A = p.xn; lda = DM; B = n2 < 32 ? p.Wk : p.Wv; ldb = 4096; k0 = 0; nkt = DM / BK; out = p.qkv; ldo = NQKV; sig = p.ctl + C_KV + m * 32;
        ocol_add = n2 < 32 ? 4096 : 8192;
      } else if (t < T_Q) {
        int u = t - T_KV; m = u >> 5; nt = u & 31; epi = 1; A = p.xn; lda = DM; B = p.Wq; ldb = 4096; k0 = 0; nkt = DM / BK; out = p.qkv; ldo = NQKV; sig = p.ctl + C_Q + m * 32;
      } else if (t < T_H) {
        int u = t - T_Q; m = u / 28; nt = u % 28; epi = 2; A = p.xn; lda = DM; B = p.w1; ldb = FF; k0 = 0; nkt = DM / BK; out = p.H; ldo = FF; sig = p.ctl + C_H + m * 32;
      } else {
        int u = t - T_H; m = u >> 3; nt = u & 7; epi = 3; A = p.H; lda = FF; B = p.w2; ldb = DM; k0 = 0; nkt = 56; out = p.acc + 4 * (size_t)S_ * DM; ldo = DM; sig = p.ctl + C_F + (2 * m * 8 + nt) * 16;
        wait_count(p.ctl + C_H + m * 32, 28, p.ctl);
      }
      gemm_tile<256>(epi, smem, A, lda, B, ldb, m * 256, nt * 128, k0, nkt, out, ldo, ocol_add + nt * 128, p.b1);
      unsigned prev = signal_count(sig, smem);
      if (epi == 3) {
        if (prev == 4) final_tile(p, 2 * m, nt);
        prev = signal_count(sig + 8 * 16, smem);
        if (prev == 4) final_tile(p, 2 * m + 1, nt);
      }
    } else {
      int u = t - T_B, mt = u / 48, v = u % 48;
      if (v < 16) {
        int s = mt * 4 + (v >> 2), hg = v & 3;
        wait_count(p.ctl + C_KV + (mt >> 1) * 32, mt < 2 ? 128 : 64, p.ctl);
        wait_count(p.ctl + C_Q + (mt >> 1) * 32, 32, p.ctl);
        attn_task(smem, p, s, hg);
        signal_count(p.ctl + C_A + mt * 32, smem);
      } else {
        int d = v - 16, nt = d >> 2, ks = d & 3;
        wait_count(p.ctl + C_A + mt * 32, 16, p.ctl);
        gemm_tile<128>(3, smem, p.O, 4096, p.Wo, DM, mt * 128, nt * 128, ks * 1024, 16, p.acc + (size_t)ks * S_ * DM, DM, nt * 128, nullptr);
        unsigned prev = signal_count(p.ctl + C_F + (mt * 8 + nt) * 16, smem);
        if (prev == 4) final_tile(p, mt, nt);
      }
    }
  }
}

extern "C" void kernel_launch(void* const* d_in, const int* in_sizes, int n_in, void* d_out, int out_size,
                              void* d_ws, size_t ws_size, hipStream_t stream) {
  char* ws = (char*)d_ws;
  MegaP p;
  p.x = (const u16*)d_in[0];
  const u16* mem0 = (const u16*)d_in[1];
  p.Wq = (const u16*)d_in[2]; p.Wk = (const u16*)d_in[3]; p.Wv = (const u16*)d_in[4]; p.Wo = (const u16*)d_in[5];
  p.w1 = (const u16*)d_in[8]; p.b1 = (const u16*)d_in[9]; p.w2 = (const u16*)d_in[10]; p.b2 = (const u16*)d_in[11];
  p.xn = (u16*)(ws + WS_XN); p.qkv = (u16*)(ws + WS_QKV); p.H = (u16*)(ws + WS_H); p.O = (u16*)(ws + WS_O);
  p.memx = (u16*)(ws + WS_MEM); p.qkvm = (u16*)(ws + WS_QKVM); p.part = (float*)(ws + WS_PART);
  p.acc = (float*)(ws + WS_ACC); p.ctl = (unsigned*)(ws + WS_TMO); p.out = (u16*)d_out;

  (void)hipFuncSetAttribute((const void*)kv_kernel, hipFuncAttributeMaxDynamicSharedMemorySize, 2 * (256 * BK * 2 + B_BYTES));
  (void)hipFuncSetAttribute((const void*)mega_kernel, hipFuncAttributeMaxDynamicSharedMemorySize, MEGA_LDS);

  (void)hipMemsetAsync(ws + WS_TMO, 0, WS_ZERO_BYTES, stream);
  ln_kernel<<<S_ / 4, 256, 0, stream>>>(p.x, mem0, p.xn, p.memx, p.qkvm, p.part, p.acc);
  mega_kernel<<<256, 768, MEGA_LDS, stream>>>(p);
}
```

```cpp
#include <hip/hip_runtime.h>
#include <hip/hip_bf16.h>

typedef unsigned short u16;
typedef unsigned long long u64;
using bf16x8 = __attribute__((ext_vector_type(8))) short;
using bf16x4 = __attribute__((ext_vector_type(4))) short;
using f32x4 = __attribute__((ext_vector_type(4))) float;
using u32x4 = __attribute__((ext_vector_type(4))) unsigned;
typedef __attribute__((address_space(3))) bf16x4 lds_b4;
typedef __attribute__((address_space(1))) unsigned long long gu64;
typedef __attribute__((address_space(1))) unsigned gu32;

constexpr int S_ = 2048, DM = 1024, NH = 32, DH = 128, MM = 8, SEG = 32, NSEG = 64, FF = 3584;
constexpr int NQKV = 12288;
constexpr float SCALE = 0.08838834764831845f;

constexpr size_t WS_TMO = 0;
constexpr size_t WS_ZERO_BYTES = 32768;
constexpr int C_TMO = 0, C_TICKET = 32, C_Q = 64, C_H = 64 + 8 * 32, C_A = 64 + 16 * 32, C_KV = 64 + 48 * 32, C_F = 2048;
constexpr size_t WS_PART = 1ull << 20;
constexpr size_t WS_PART_BYTES = 4ull * 4 * 32 * 32 * 64 * 4;
constexpr size_t WS_XN = 8ull << 20;
constexpr size_t WS_QKV = 16ull << 20;
constexpr size_t WS_H = 72ull << 20;
constexpr size_t WS_O = 96ull << 20;
constexpr size_t WS_MEM = 120ull << 20;
constexpr size_t WS_QKVM = 122ull << 20;
constexpr size_t WS_ACC = 140ull << 20;

__device__ __forceinline__ float bf2f(u16 v) { return __uint_as_float(((unsigned)v) << 16); }
__device__ __forceinline__ u16 f2bf(float f) {
  __hip_bfloat16 h = __float2bfloat16(f);
  return *reinterpret_cast<u16*>(&h);
}
__device__ __forceinline__ float rbf(float f) { return bf2f(f2bf(f)); }
__device__ __forceinline__ unsigned pack2(float a, float b) {
  unsigned r = (unsigned)f2bf(a) | ((unsigned)f2bf(b) << 16);
  return r == 0xFFFFFFFFu ? 0x7FFF7FFFu : r;
}

__global__ __launch_bounds__(256) void ln_kernel(const u16* __restrict__ x, const u16* __restrict__ mem0,
                                                  u16* __restrict__ xn, u16* memx, u16* qkvm, float* part, float* acc) {
  int wave = threadIdx.x >> 6, lane = threadIdx.x & 63;
  int row = blockIdx.x * 4 + wave;
  const u16* xr = x + (size_t)row * DM;
  float v[16];
  bf16x8 a = *reinterpret_cast<const bf16x8*>(xr + lane * 8);
  bf16x8 b = *reinterpret_cast<const bf16x8*>(xr + 512 + lane * 8);
  float s = 0.f;
  for (int i = 0; i < 8; i++) { v[i] = bf2f((u16)a[i]); v[8 + i] = bf2f((u16)b[i]); s += v[i] + v[8 + i]; }
  for (int o = 32; o; o >>= 1) s += __shfl_xor(s, o);
  float m = s * (1.f / DM);
  float q = 0.f;
  for (int i = 0; i < 16; i++) { float d = v[i] - m; q += d * d; }
  for (int o = 32; o; o >>= 1) q += __shfl_xor(q, o);
  float r = rsqrtf(q * (1.f / DM) + 1e-5f);
  bf16x8 oa, ob;
  for (int i = 0; i < 8; i++) { oa[i] = (short)f2bf((v[i] - m) * r); ob[i] = (short)f2bf((v[8 + i] - m) * r); }
  *reinterpret_cast<bf16x8*>(xn + (size_t)row * DM + lane * 8) = oa;
  *reinterpret_cast<bf16x8*>(xn + (size_t)row * DM + 512 + lane * 8) = ob;
  size_t tid = (size_t)blockIdx.x * 256 + threadIdx.x, nth = (size_t)gridDim.x * 256;
  u32x4 P = {0xFFFFFFFFu, 0xFFFFFFFFu, 0xFFFFFFFFu, 0xFFFFFFFFu};
  for (size_t i = tid; i < (size_t)NSEG * MM * DM / 8; i += nth) {
    u32x4 val = P;
    if (i < MM * DM / 8) { int blk = (int)i >> 3, rw = (int)i & 7; val = reinterpret_cast<const u32x4*>(mem0)[rw * 128 + blk]; }
    reinterpret_cast<u32x4*>(memx)[i] = val;
  }
  for (size_t i = tid; i < (size_t)NSEG * MM * NQKV / 8; i += nth) reinterpret_cast<u32x4*>(qkvm)[i] = P;
  for (size_t i = tid; i < WS_PART_BYTES / 16; i += nth) reinterpret_cast<u32x4*>(part)[i] = P;
}

constexpr int BN = 128, BK = 64;
constexpr int B_BYTES = BK * BN * 2;

__device__ __forceinline__ float gelu_tanh(float x) {
  float u = 0.7978845608028654f * (x + 0.044715f * x * x * x);
  float t = 1.f - 2.f / (__expf(2.f * u) + 1.f);
  return 0.5f * x * (1.f + t);
}
__device__ __forceinline__ void st8_sc1(void* p, bf16x4 v) {
  asm volatile("global_store_dwordx2 %0, %1, off sc1\n\ts_nop 1" ::"v"(p), "v"(v) : "memory");
}

template <int BMT>
__device__ __forceinline__ void gemm_tile(const int EPI, char* smem, const u16* __restrict__ A, int lda, const u16* __restrict__ B, int ldb,
                                          int m0, int bcol, int k0, int nkt, void* out, int ldo, int ocol, const u16* bias) {
  constexpr int A_BYTES = BMT * BK * 2, STAGE = A_BYTES + B_BYTES;
  constexpr int WN = BMT == 256 ? 2 : 4, NT = BMT == 256 ? 4 : 2;
  int tid = threadIdx.x;
  asm volatile("" : "+v"(tid));
  const int wid = __builtin_amdgcn_readfirstlane(tid >> 6), lane = tid & 63, fr = lane & 15, fq = lane >> 4;
  const int wr = wid / WN, wc = wid % WN;
  auto stage = [&](int buf, int kt) {
    char* sa = smem + buf * STAGE;
    char* sb = sa + A_BYTES;
    int kk = k0 + kt * BK;
#pragma unroll
    for (int i = 0; i < BMT / 64; i++) {
      int pb = (wid * (BMT / 64) + i) * 1024 + lane * 16;
      int row = pb >> 7, chs = (pb >> 4) & 7, ch = chs ^ ((row >> 1) & 7);
      __builtin_amdgcn_global_load_lds((const unsigned*)(A + (size_t)(m0 + row) * lda + kk + ch * 8),
                                       (unsigned*)(sa + (wid * (BMT / 64) + i) * 1024), 16, 0, 0);
    }
#pragma unroll
    for (int i = 0; i < 2; i++) {
      int pb = (wid * 2 + i) * 1024 + lane * 16;
      int row = pb >> 8, chs = (pb >> 4) & 15, ch = chs ^ (((row & 3) << 2) | ((row >> 2) & 3));
      __builtin_amdgcn_global_load_lds((const unsigned*)(B + (size_t)(kk + row) * ldb + bcol + ch * 8),
                                       (unsigned*)(sb + (wid * 2 + i) * 1024), 16, 0, 0);
    }
  };
  f32x4 acc[4][NT];
#pragma unroll
  for (int i = 0; i < 4; i++)
#pragma unroll
    for (int j = 0; j < NT; j++) acc[i][j] = f32x4{0.f, 0.f, 0.f, 0.f};
  __syncthreads();
  stage(0, 0);
  stage(1, 1);
  int cur = 0;
  for (int t = 0; t < nkt; t++) {
    if (t + 1 < nkt) { if (BMT == 256) asm volatile("s_waitcnt vmcnt(6) lgkmcnt(0)" ::: "memory"); else asm volatile("s_waitcnt vmcnt(4) lgkmcnt(0)" ::: "memory"); }
    else asm volatile("s_waitcnt vmcnt(0) lgkmcnt(0)" ::: "memory");
    __builtin_amdgcn_s_barrier();
    asm volatile("" ::: "memory");
    int nxt = cur == 0 ? 2 : cur - 1;
    if (t + 2 < nkt) stage(nxt, t + 2);
    char* sa = smem + cur * STAGE;
    char* sb = sa + A_BYTES;
    cur = cur == 2 ? 0 : cur + 1;
#pragma unroll
    for (int ks = 0; ks < 2; ks++) {
      bf16x8 af[4], bfr[NT];
#pragma unroll
      for (int m = 0; m < 4; m++) {
        int row = wr * 64 + m * 16 + fr, ch = ks * 4 + fq;
        af[m] = *reinterpret_cast<const bf16x8*>(sa + row * 128 + 16 * (ch ^ ((row >> 1) & 7)));
      }
#pragma unroll
      for (int n = 0; n < NT; n++) {
        int c = wc * NT + n;
        int q = fr >> 2, pp = fr & 3;
        bf16x4 lo, hi;
        {
          int row = ks * 32 + 8 * fq + q, ch = 2 * c + (pp >> 1);
          int off = 256 * row + 16 * (ch ^ (((row & 3) << 2) | ((row >> 2) & 3))) + 8 * (pp & 1);
          lo = __builtin_amdgcn_ds_read_tr16_b64_v4i16((lds_b4*)(sb + off));
        }
        {
          int row = ks * 32 + 8 * fq + 4 + q, ch = 2 * c + (pp >> 1);
          int off = 256 * row + 16 * (ch ^ (((row & 3) << 2) | ((row >> 2) & 3))) + 8 * (pp & 1);
          hi = __builtin_amdgcn_ds_read_tr16_b64_v4i16((lds_b4*)(sb + off));
        }
        bfr[n] = bf16x8{lo[0], lo[1], lo[2], lo[3], hi[0], hi[1], hi[2], hi[3]};
      }
#pragma unroll
      for (int m = 0; m < 4; m++)
#pragma unroll
        for (int n = 0; n < NT; n++)
          acc[m][n] = __builtin_amdgcn_mfma_f32_16x16x32_bf16(bfr[n], af[m], acc[m][n], 0, 0, 0);
    }
  }
#pragma unroll
  for (int m = 0; m < 4; m++) {
    int row = m0 + wr * 64 + m * 16 + fr;
#pragma unroll
    for (int n = 0; n < NT; n++) {
      int col = ocol + (wc * NT + n) * 16 + fq * 4;
      if (EPI == 0 || EPI == 1) {
        bf16x4 o;
#pragma unroll
        for (int r = 0; r < 4; r++) o[r] = (short)f2bf(acc[m][n][r]);
        u16* dst = (u16*)out + (size_t)row * ldo + col;
        if (EPI == 0) *reinterpret_cast<bf16x4*>(dst) = o; else st8_sc1(dst, o);
      } else if (EPI == 2) {
        bf16x4 o, bb = *reinterpret_cast<const bf16x4*>(bias + col);
#pragma unroll
        for (int r = 0; r < 4; r++) o[r] = (short)f2bf(gelu_tanh(rbf(rbf(acc[m][n][r]) + bf2f((u16)bb[r]))));
        st8_sc1((u16*)out + (size_t)row * ldo + col, o);
      } else {
        float* dst = (float*)out + (size_t)row * ldo + col;
        asm volatile("global_store_dwordx4 %0, %1, off sc1\n\ts_nop 1" ::"v"(dst), "v"(acc[m][n]) : "memory");
      }
    }
  }
}

constexpr int KROW = 272;
constexpr int R_RED = 0;
constexpr int R_KT = 24576;
constexpr int R_VT = R_KT + 32 * KROW;
constexpr int R_RED2 = R_VT + 32 * KROW;
constexpr int R_X = R_RED2 + 2048;
constexpr int R_STG = R_X + 6144;
constexpr int R_WO = R_STG;
constexpr int R_LDS = R_STG + 12 * 8192;
#define RED_DELAY 8
#define DLY_B 0
#define DLY_A 0
#define STAMP(i)
#define ST2(i)

__device__ __forceinline__ float xor16(float x) {
  unsigned u = __float_as_uint(x);
  auto r = __builtin_amdgcn_permlane16_swap(u, u, false, false);
  unsigned a = r[0], b = r[1];
  return __uint_as_float(((threadIdx.x >> 4) & 1) ? a : b);
}
__device__ __forceinline__ float xor32(float x) {
  unsigned u = __float_as_uint(x);
  auto r = __builtin_amdgcn_permlane32_swap(u, u, false, false);
  unsigned a = r[0], b = r[1];
  return __uint_as_float(((threadIdx.x >> 5) & 1) ? a : b);
}
__device__ __forceinline__ bool poisoned(const u32x4& v) {
  return (v[0] == 0xFFFFFFFFu) | (v[1] == 0xFFFFFFFFu) | (v[2] == 0xFFFFFFFFu) | (v[3] == 0xFFFFFFFFu);
}
using u32x2 = __attribute__((ext_vector_type(2))) unsigned;

__device__ __forceinline__ void rec_role(char* smem, const u16* __restrict__ Wq, const u16* __restrict__ Wk,
                                         const u16* __restrict__ Wv, const u16* __restrict__ Wo,
                                         const u16* __restrict__ qkvt, u16* memx, u16* qkvm,
                                         float* part, unsigned* tmo) {
  const int tid = threadIdx.x, w = __builtin_amdgcn_readfirstlane(tid >> 6), lane = tid & 63, fr = lane & 15, g = lane >> 4;
  const int rb = blockIdx.x;
  const int h = rb >> 2, j = rb & 3;
  const unsigned spin_cap = 4000000;
  char* stg = smem + R_STG + w * 8192;
  if (tid < 8) *reinterpret_cast<volatile unsigned*>(smem + R_RED2 + 1024 + tid * 4) = 0u;
  auto stage_tokens = [&](int sn) {
    if ((sn & 7) == 0) {
      unsigned spins = 0;
      while (__hip_atomic_load((gu32*)(tmo + C_KV + (sn >> 3) * 32), __ATOMIC_RELAXED, __HIP_MEMORY_SCOPE_AGENT) < (sn == 0 ? 128u : 64u)) {
        __builtin_amdgcn_s_sleep(8);
        if (++spins > 40000000u) { __hip_atomic_store((gu32*)tmo, 7u, __ATOMIC_RELAXED, __HIP_MEMORY_SCOPE_AGENT); break; }
      }
    }
    int t0 = tid - 640;
    asm volatile("" : "+v"(t0));
    const u16* sb = qkvt + (size_t)(sn * SEG + (t0 >> 4)) * NQKV + 4096 + h * 128 + (t0 & 15) * 8;
    const u16* s1 = sb + (size_t)8 * NQKV; const u16* s2 = sb + (size_t)16 * NQKV; const u16* s3 = sb + (size_t)24 * NQKV;
    u32x4 v0, v1, v2, v3, v4, v5, v6, v7;
    asm volatile("global_load_dwordx4 %0, %8, off sc1\n\tglobal_load_dwordx4 %1, %9, off sc1\n\t"
                 "global_load_dwordx4 %2, %10, off sc1\n\tglobal_load_dwordx4 %3, %11, off sc1\n\t"
                 "global_load_dwordx4 %4, %12, off sc1\n\tglobal_load_dwordx4 %5, %13, off sc1\n\t"
                 "global_load_dwordx4 %6, %14, off sc1\n\tglobal_load_dwordx4 %7, %15, off sc1\n\ts_waitcnt vmcnt(0)"
                 : "=&v"(v0), "=&v"(v1), "=&v"(v2), "=&v"(v3), "=&v"(v4), "=&v"(v5), "=&v"(v6), "=&v"(v7)
                 : "v"(sb), "v"(s1), "v"(s2), "v"(s3), "v"(sb + 4096), "v"(s1 + 4096), "v"(s2 + 4096), "v"(s3 + 4096) : "memory");
    char* dk = smem + R_KT + (t0 >> 4) * KROW + (t0 & 15) * 16;
    char* dv = smem + R_VT + (t0 >> 4) * KROW + (t0 & 15) * 16;
    *reinterpret_cast<u32x4*>(dk) = v0; *reinterpret_cast<u32x4*>(dk + 8 * KROW) = v1;
    *reinterpret_cast<u32x4*>(dk + 16 * KROW) = v2; *reinterpret_cast<u32x4*>(dk + 24 * KROW) = v3;
    *reinterpret_cast<u32x4*>(dv) = v4; *reinterpret_cast<u32x4*>(dv + 8 * KROW) = v5;
    *reinterpret_cast<u32x4*>(dv + 16 * KROW) = v6; *reinterpret_cast<u32x4*>(dv + 24 * KROW) = v7;
  };

  if (w < 4) {
    __builtin_amdgcn_s_setprio(3);
    bf16x8 wo[4][4];
#pragma unroll
    for (int half = 0; half < 2; half++) {
#pragma unroll
      for (int i = 0; i < 8; i++) {
        int c = i * 64 + lane, row = c >> 2, ch = c & 3;
        u32x4 v = *reinterpret_cast<const u32x4*>(Wo + (size_t)(h * 128 + row) * DM + 256 * j + 64 * w + 32 * half + ch * 8);
        *reinterpret_cast<u32x4*>(stg + row * 64 + ch * 16) = v;
      }
      __builtin_amdgcn_s_waitcnt(0);
      __builtin_amdgcn_wave_barrier();
#pragma unroll
      for (int t2 = 0; t2 < 2; t2++)
#pragma unroll
        for (int kk = 0; kk < 4; kk++) {
          int q = fr >> 2, pp = fr & 3;
          bf16x4 lo = __builtin_amdgcn_ds_read_tr16_b64_v4i16((lds_b4*)(stg + (kk * 32 + 4 * g + q) * 64 + t2 * 32 + pp * 8));
          bf16x4 hi = __builtin_amdgcn_ds_read_tr16_b64_v4i16((lds_b4*)(stg + (kk * 32 + 16 + 4 * g + q) * 64 + t2 * 32 + pp * 8));
          wo[half * 2 + t2][kk] = bf16x8{lo[0], lo[1], lo[2], lo[3], hi[0], hi[1], hi[2], hi[3]};
        }
      __builtin_amdgcn_s_waitcnt(0);
      __builtin_amdgcn_wave_barrier();
    }
    __syncthreads();
#pragma unroll
    for (int t4 = 0; t4 < 4; t4++)
#pragma unroll
      for (int kk = 0; kk < 4; kk++) *reinterpret_cast<bf16x8*>(smem + R_WO + w * 16384 + ((t4 * 4 + kk) * 64 + lane) * 16) = wo[t4][kk];
    for (int s = 0; s < NSEG; s++) {
      __syncthreads();
      bf16x8 kt[2][4], vt[8];
      {
        const char* kb = smem + R_KT;
        const char* vb = smem + R_VT;
#pragma unroll
        for (int t = 0; t < 2; t++)
#pragma unroll
          for (int ks = 0; ks < 4; ks++) kt[t][ks] = *reinterpret_cast<const bf16x8*>(kb + (16 * t + fr) * KROW + ks * 64 + g * 16);
        int q = fr >> 2, pp = fr & 3;
#pragma unroll
        for (int et = 0; et < 8; et++) {
          bf16x4 lo = __builtin_amdgcn_ds_read_tr16_b64_v4i16((lds_b4*)(vb + (4 * g + q) * KROW + et * 32 + pp * 8));
          bf16x4 hi = __builtin_amdgcn_ds_read_tr16_b64_v4i16((lds_b4*)(vb + (16 + 4 * g + q) * KROW + et * 32 + pp * 8));
          vt[et] = bf16x8{lo[0], lo[1], lo[2], lo[3], hi[0], hi[1], hi[2], hi[3]};
        }
      }
      STAMP(3)
      __syncthreads();
      u32x4 q0, q1, q2, q3, k0, k1, k2, k3;
      u32x2 v0, v1, v2, v3, v4, v5, v6, v7;
      {
        const char* qp = smem + R_X + ((g >> 1) * 128 + (fr & 7) * 16 + (g & 1) * 8) * 2;
        const char* vp = smem + R_X + (512 + fr * 8 + (g & 1) * 4) * 2;
        q0 = *reinterpret_cast<const u32x4*>(qp); q1 = *reinterpret_cast<const u32x4*>(qp + 1536);
        q2 = *reinterpret_cast<const u32x4*>(qp + 3072); q3 = *reinterpret_cast<const u32x4*>(qp + 4608);
        k0 = *reinterpret_cast<const u32x4*>(qp + 512); k1 = *reinterpret_cast<const u32x4*>(qp + 2048);
        k2 = *reinterpret_cast<const u32x4*>(qp + 3584); k3 = *reinterpret_cast<const u32x4*>(qp + 5120);
        v0 = *reinterpret_cast<const u32x2*>(vp); v1 = *reinterpret_cast<const u32x2*>(vp + 256);
        v2 = *reinterpret_cast<const u32x2*>(vp + 1536); v3 = *reinterpret_cast<const u32x2*>(vp + 1792);
        v4 = *reinterpret_cast<const u32x2*>(vp + 3072); v5 = *reinterpret_cast<const u32x2*>(vp + 3328);
        v6 = *reinterpret_cast<const u32x2*>(vp + 4608); v7 = *reinterpret_cast<const u32x2*>(vp + 4864);
        asm volatile("" : "+v"(q0), "+v"(q1), "+v"(q2), "+v"(q3), "+v"(k0), "+v"(k1), "+v"(k2), "+v"(k3),
                     "+v"(v0), "+v"(v1), "+v"(v2), "+v"(v3), "+v"(v4), "+v"(v5), "+v"(v6), "+v"(v7));
      }
      STAMP(4)
      f32x4 st[3];
      {
        bf16x8 qf[4] = {__builtin_bit_cast(bf16x8, q0), __builtin_bit_cast(bf16x8, q1), __builtin_bit_cast(bf16x8, q2), __builtin_bit_cast(bf16x8, q3)};
        bf16x8 km[4] = {__builtin_bit_cast(bf16x8, k0), __builtin_bit_cast(bf16x8, k1), __builtin_bit_cast(bf16x8, k2), __builtin_bit_cast(bf16x8, k3)};
#pragma unroll
        for (int t = 0; t < 3; t++) st[t] = f32x4{0.f, 0.f, 0.f, 0.f};
#pragma unroll
        for (int ks = 0; ks < 4; ks++) {
          st[0] = __builtin_amdgcn_mfma_f32_16x16x32_bf16(kt[0][ks], qf[ks], st[0], 0, 0, 0);
          st[1] = __builtin_amdgcn_mfma_f32_16x16x32_bf16(kt[1][ks], qf[ks], st[1], 0, 0, 0);
          st[2] = __builtin_amdgcn_mfma_f32_16x16x32_bf16(km[ks], qf[ks], st[2], 0, 0, 0);
        }
      }
      bf16x8 wf[4][4];
      asm volatile("" ::: "memory");
#pragma unroll
      for (int t4 = 0; t4 < 4; t4++)
#pragma unroll
        for (int kk = 0; kk < 4; kk++) wf[t4][kk] = *reinterpret_cast<const bf16x8*>(smem + R_WO + w * 16384 + ((t4 * 4 + kk) * 64 + lane) * 16);
      asm volatile("" ::: "memory");
      if (g >= 2) st[2] = f32x4{-3.0e38f, -3.0e38f, -3.0e38f, -3.0e38f};
      float mx = fmaxf(fmaxf(fmaxf(st[0][0], st[0][1]), fmaxf(st[0][2], st[0][3])), fmaxf(fmaxf(st[1][0], st[1][1]), fmaxf(st[1][2], st[1][3])));
      mx = fmaxf(mx, fmaxf(fmaxf(st[2][0], st[2][1]), fmaxf(st[2][2], st[2][3])));
      mx = fmaxf(mx, xor16(mx));
      mx = fmaxf(mx, xor32(mx));
      const float c2 = SCALE * 1.4426950408889634f;
      float mc = mx * c2;
      float sum = 0.f;
#pragma unroll
      for (int t = 0; t < 3; t++)
#pragma unroll
        for (int r = 0; r < 4; r++) {
          float e = __builtin_amdgcn_exp2f(__builtin_fmaf(st[t][r], c2, -mc));
          st[t][r] = e;
          sum += e;
        }
      sum += xor16(sum);
      sum += xor32(sum);
      float inv = __builtin_amdgcn_rcpf(sum);
      bf16x8 pf[2];
#pragma unroll
      for (int r = 0; r < 4; r++) {
        pf[0][r] = (short)f2bf(st[0][r] * inv);
        pf[0][4 + r] = (short)f2bf(st[1][r] * inv);
        pf[1][r] = (short)f2bf(st[2][r] * inv);
        pf[1][4 + r] = 0;
      }
      bf16x8 of[4];
      {
        u32x2 vv[8] = {v0, v1, v2, v3, v4, v5, v6, v7};
#pragma unroll
        for (int et = 0; et < 8; et++) {
          f32x4 o = {0.f, 0.f, 0.f, 0.f};
          o = __builtin_amdgcn_mfma_f32_16x16x32_bf16(vt[et], pf[0], o, 0, 0, 0);
          u32x4 vx = {vv[et][0], vv[et][1], 0u, 0u};
          o = __builtin_amdgcn_mfma_f32_16x16x32_bf16(__builtin_bit_cast(bf16x8, vx), pf[1], o, 0, 0, 0);
#pragma unroll
          for (int r = 0; r < 4; r++) of[et >> 1][(et & 1) * 4 + r] = (short)f2bf(o[r]);
        }
      }
      if (s + 1 < NSEG) {
        f32x4 pa[4];
#pragma unroll
        for (int t4 = 0; t4 < 4; t4++) pa[t4] = f32x4{0.f, 0.f, 0.f, 0.f};
#pragma unroll
        for (int kk = 0; kk < 4; kk++)
#pragma unroll
          for (int t4 = 0; t4 < 4; t4++) pa[t4] = __builtin_amdgcn_mfma_f32_16x16x32_bf16(of[kk], wf[t4][kk], pa[t4], 0, 0, 0);
        if (g < 2) {
#pragma unroll
          for (int t4 = 0; t4 < 4; t4++) {
            int col = 64 * w + 16 * t4 + fr;
            int dst = col >> 3, cc = col & 7;
            float* base = part + ((((size_t)(s & 3) * 4 + j) * 32 + dst) * 32 + h) * 64 + cc * 8 + 4 * g;
            u32x4 vv;
#pragma unroll
            for (int r = 0; r < 4; r++) { unsigned u = __float_as_uint(pa[t4][r]); vv[r] = u == 0xFFFFFFFFu ? 0x7FFFFFFFu : u; }
            asm volatile("global_store_dwordx4 %0, %1, off sc1\n\ts_nop 1" :: "v"(base), "v"(vv) : "memory");
          }
        }
      }
      STAMP(5)
    }
  } else {
    const int gw = w - 4;
    bf16x8 wb[6][4];
#pragma unroll
    for (int mat = 0; mat < 3; mat++) {
      const u16* W = mat == 0 ? Wq : (mat == 1 ? Wk : Wv);
#pragma unroll
      for (int i = 0; i < 8; i++) {
        int c = i * 64 + lane, row = c >> 2, ch = c & 3;
        u32x4 v = *reinterpret_cast<const u32x4*>(W + (size_t)(128 * gw + row) * 4096 + h * 128 + 32 * j + ch * 8);
        *reinterpret_cast<u32x4*>(stg + row * 64 + ch * 16) = v;
      }
      __builtin_amdgcn_s_waitcnt(0);
      __builtin_amdgcn_wave_barrier();
#pragma unroll
      for (int t2 = 0; t2 < 2; t2++)
#pragma unroll
        for (int ks = 0; ks < 4; ks++) {
          int q = fr >> 2, pp = fr & 3;
          bf16x4 lo = __builtin_amdgcn_ds_read_tr16_b64_v4i16((lds_b4*)(stg + (ks * 32 + 8 * g + q) * 64 + t2 * 32 + pp * 8));
          bf16x4 hi = __builtin_amdgcn_ds_read_tr16_b64_v4i16((lds_b4*)(stg + (ks * 32 + 8 * g + 4 + q) * 64 + t2 * 32 + pp * 8));
          wb[mat * 2 + t2][ks] = bf16x8{lo[0], lo[1], lo[2], lo[3], hi[0], hi[1], hi[2], hi[3]};
        }
      __builtin_amdgcn_s_waitcnt(0);
      __builtin_amdgcn_wave_barrier();
    }
    if (gw >= 6) stage_tokens(0);
    __syncthreads();
    for (int s = 0; s < NSEG; s++) {
      if (s > 0) { volatile unsigned* pflag = reinterpret_cast<volatile unsigned*>(smem + R_RED2 + 1028); while (*pflag != (unsigned)s) __builtin_amdgcn_s_sleep(2); }
      if (gw == 0) { ST2(0) }
      for (int i = 0; i < DLY_A; i++) __builtin_amdgcn_s_sleep(1);
      u32x4 a0, a1, a2, a3;
      {
        const u16* mp = memx + (size_t)s * MM * DM + (16 * gw + g) * 64 + (fr & 7) * 8;
        unsigned spins = 0;
        while (true) {
          asm volatile(
              "global_load_dwordx4 %0, %4, off sc1\n\t"
              "global_load_dwordx4 %1, %4, off offset:512 sc1\n\t"
              "global_load_dwordx4 %2, %4, off offset:1024 sc1\n\t"
              "global_load_dwordx4 %3, %4, off offset:1536 sc1\n\t"
              "s_waitcnt vmcnt(0)"
              : "=&v"(a0), "=&v"(a1), "=&v"(a2), "=&v"(a3) : "v"(mp) : "memory");
          bool bad = poisoned(a0) | poisoned(a1) | poisoned(a2) | poisoned(a3);
          if (!__any(bad)) break;
          if (++spins > spin_cap) { if (lane == 0) __hip_atomic_store((gu32*)tmo, 1u, __ATOMIC_RELAXED, __HIP_MEMORY_SCOPE_AGENT); break; }
        }
      }
      if (gw == 0) { ST2(1) }
      {
        bf16x8 af[4] = {__builtin_bit_cast(bf16x8, a0), __builtin_bit_cast(bf16x8, a1), __builtin_bit_cast(bf16x8, a2), __builtin_bit_cast(bf16x8, a3)};
        f32x4 acc[6];
#pragma unroll
        for (int t = 0; t < 6; t++) acc[t] = f32x4{0.f, 0.f, 0.f, 0.f};
#pragma unroll
        for (int ks = 0; ks < 4; ks++)
#pragma unroll
          for (int t = 0; t < 6; t++) acc[t] = __builtin_amdgcn_mfma_f32_16x16x32_bf16(af[ks], wb[t][ks], acc[t], 0, 0, 0);
        if (g < 2) {
          float* red = reinterpret_cast<float*>(smem + R_RED) + gw * 768;
#pragma unroll
          for (int t = 0; t < 4; t++)
#pragma unroll
            for (int r = 0; r < 4; r++) red[t * 128 + (4 * g + r) * 16 + fr] = acc[t][r];
#pragma unroll
          for (int t = 4; t < 6; t++) *reinterpret_cast<f32x4*>(red + t * 128 + fr * 8 + 4 * g) = acc[t];
        }
      }
      if (gw == 0) { ST2(2) }
      __syncthreads();
      if (gw < 2) {
        if (gw == 0 || lane < 32) {
          int off = gw * 512 + lane * 8;
          const float* red = reinterpret_cast<const float*>(smem + R_RED) + off;
          f32x4 x0 = {0.f, 0.f, 0.f, 0.f}, x1 = x0;
#pragma unroll
          for (int ww = 0; ww < 8; ww++) {
            x0 += *reinterpret_cast<const f32x4*>(red + ww * 768);
            x1 += *reinterpret_cast<const f32x4*>(red + ww * 768 + 4);
          }
          u32x4 v4 = {pack2(x0[0], x0[1]), pack2(x0[2], x0[3]), pack2(x1[0], x1[1]), pack2(x1[2], x1[3])};
          u16* dst = qkvm + (((size_t)s * NH + h) * 4 + j) * 768 + off;
          asm volatile("global_store_dwordx4 %0, %1, off sc1\n\ts_nop 1" ::"v"(dst), "v"(v4) : "memory");
        }
      } else {
        int c = tid - 384;
        const u16* src = qkvm + ((size_t)s * NH + h) * 3072 + c * 8;
        unsigned spins = 0;
        for (int i = 0; i < DLY_B; i++) __builtin_amdgcn_s_sleep(1);
        u32x4 v;
        while (true) {
          asm volatile("global_load_dwordx4 %0, %1, off sc1\n\ts_waitcnt vmcnt(0)" : "=&v"(v) : "v"(src) : "memory");
          if (!__any(poisoned(v))) break;
          if (++spins > spin_cap) { if (lane == 0) __hip_atomic_store((gu32*)tmo, 2u, __ATOMIC_RELAXED, __HIP_MEMORY_SCOPE_AGENT); break; }
        }
        *reinterpret_cast<u32x4*>(smem + R_X + c * 16) = v;
      }
      __syncthreads();
      if (gw < 2) {
      } else if (gw < 4) {
        if (s > 0) {
          int ln = lane; asm volatile("" : "+v"(ln));
          float* base = part + ((((size_t)((s - 1) & 3) * 4 + j) * 32 + h) * 32 + 16 * (gw - 2)) * 64 + ln * 4;
          u32x4 P = {0xFFFFFFFFu, 0xFFFFFFFFu, 0xFFFFFFFFu, 0xFFFFFFFFu};
          asm volatile(
              "global_store_dwordx4 %0, %1, off sc1\n\t"
              "global_store_dwordx4 %0, %1, off offset:1024 sc1\n\t"
              "global_store_dwordx4 %0, %1, off offset:2048 sc1\n\t"
              "global_store_dwordx4 %0, %1, off offset:3072 sc1\n\ts_nop 1" ::"v"(base), "v"(P) : "memory");
        }
      } else if (gw < 6) {
        if (s + 1 < NSEG) {
          int ln = lane; asm volatile("" : "+v"(ln));
          float* base = part + ((((size_t)(s & 3) * 4 + j) * 32 + h) * 32 + 16 * (gw - 4)) * 64 + ln * 4;
          unsigned spins = 0;
          u32x4 b0, b1, b2, b3;
          for (int i = 0; i < RED_DELAY; i++) __builtin_amdgcn_s_sleep(1);
          while (true) {
            asm volatile(
                "global_load_dwordx4 %0, %4, off sc1\n\t"
                "global_load_dwordx4 %1, %4, off offset:1024 sc1\n\t"
                "global_load_dwordx4 %2, %4, off offset:2048 sc1\n\t"
                "global_load_dwordx4 %3, %4, off offset:3072 sc1\n\t"
                "s_waitcnt vmcnt(0)"
                : "=&v"(b0), "=&v"(b1), "=&v"(b2), "=&v"(b3) : "v"(base) : "memory");
            bool bad = poisoned(b0) | poisoned(b1) | poisoned(b2) | poisoned(b3);
            if (!__any(bad)) break;
            if (++spins > spin_cap) { if (lane == 0) __hip_atomic_store((gu32*)tmo, 3u, __ATOMIC_RELAXED, __HIP_MEMORY_SCOPE_AGENT); break; }
          }
          f32x4 val = (__builtin_bit_cast(f32x4, b0) + __builtin_bit_cast(f32x4, b1)) + (__builtin_bit_cast(f32x4, b2) + __builtin_bit_cast(f32x4, b3));
#pragma unroll
          for (int i = 0; i < 4; i++) { val[i] += xor16(val[i]); val[i] += xor32(val[i]); }
          float* r2 = reinterpret_cast<float*>(smem + R_RED2) + (gw - 4) * 64;
          volatile unsigned* lflag = reinterpret_cast<volatile unsigned*>(smem + R_RED2 + 1024);
          if (lane < 16) {
#pragma unroll
            for (int i = 0; i < 4; i++) r2[(4 * (fr & 1) + i) * 8 + (fr >> 1)] = val[i];
          }
          __builtin_amdgcn_s_waitcnt(0xc07f);
          __builtin_amdgcn_wave_barrier();
          if (gw == 5) {
            if (lane == 0) *lflag = (unsigned)(s + 1);
          } else {
            while (*lflag != (unsigned)(s + 1)) {}
            if (lane < 8) {
              const float* q2 = reinterpret_cast<const float*>(smem + R_RED2) + lane * 8;
              f32x4 x0 = *reinterpret_cast<const f32x4*>(q2) + *reinterpret_cast<const f32x4*>(q2 + 64);
              f32x4 x1 = *reinterpret_cast<const f32x4*>(q2 + 4) + *reinterpret_cast<const f32x4*>(q2 + 68);
              u32x4 v4 = {pack2(x0[0], x0[1]), pack2(x0[2], x0[3]), pack2(x1[0], x1[1]), pack2(x1[2], x1[3])};
              u16* dst = memx + (size_t)(s + 1) * MM * DM + (32 * j + h) * 64 + ln * 8;
              asm volatile("global_store_dwordx4 %0, %1, off sc1\n\ts_nop 1" ::"v"(dst), "v"(v4) : "memory");
            }
            if (lane == 0) *reinterpret_cast<volatile unsigned*>(smem + R_RED2 + 1028) = (unsigned)(s + 1);
          }
        }
      } else {
        if (s + 1 < NSEG) stage_tokens(s + 1);
      }
    }
  }
}

struct MegaP {
  const u16* x; const u16* Wq; const u16* Wk; const u16* Wv; const u16* Wo; const u16* w1; const u16* b1; const u16* w2; const u16* b2;
  u16* xn; u16* qkv; u16* H; u16* O; u16* memx; u16* qkvm; float* part; float* acc; unsigned* ctl; u16* out;
};

__device__ __forceinline__ void wait_count(unsigned* word, unsigned target, unsigned* tmo) {
  if (threadIdx.x == 0) {
    unsigned spins = 0;
    while (__hip_atomic_load((gu32*)word, __ATOMIC_RELAXED, __HIP_MEMORY_SCOPE_AGENT) < target) {
      __builtin_amdgcn_s_sleep(8);
      if (++spins > 40000000u) { __hip_atomic_store((gu32*)tmo, 9u, __ATOMIC_RELAXED, __HIP_MEMORY_SCOPE_AGENT); break; }
    }
    __builtin_amdgcn_fence(__ATOMIC_ACQUIRE, "agent");
    asm volatile("s_waitcnt vmcnt(0)" ::: "memory");
  }
  __syncthreads();
}
__device__ __forceinline__ unsigned signal_count(unsigned* word, char* smem) {
  asm volatile("s_waitcnt vmcnt(0)" ::: "memory");
  __syncthreads();
  volatile unsigned* bc = reinterpret_cast<volatile unsigned*>(smem + 150 * 1024);
  if (threadIdx.x == 0) *bc = __hip_atomic_fetch_add((gu32*)word, 1u, __ATOMIC_RELAXED, __HIP_MEMORY_SCOPE_AGENT);
  __syncthreads();
  return *bc;
}

__device__ __forceinline__ void final_tile(const MegaP& p, int mt, int nt) {
  int tid = threadIdx.x;
  asm volatile("" : "+v"(tid));
  constexpr size_t SL = (size_t)S_ * DM;
#pragma unroll
  for (int i = 0; i < 8; i += 2) {
    size_t e0 = (size_t)(mt * 128 + i * 16 + (tid >> 5)) * DM + nt * 128 + (tid & 31) * 4, e1 = e0 + 16 * DM;
    f32x4 a[5], b[5];
    asm volatile("global_load_dwordx4 %0, %10, off sc1\n\tglobal_load_dwordx4 %1, %11, off sc1\n\tglobal_load_dwordx4 %2, %12, off sc1\n\t"
                 "global_load_dwordx4 %3, %13, off sc1\n\tglobal_load_dwordx4 %4, %14, off sc1\n\t"
                 "global_load_dwordx4 %5, %15, off sc1\n\tglobal_load_dwordx4 %6, %16, off sc1\n\tglobal_load_dwordx4 %7, %17, off sc1\n\t"
                 "global_load_dwordx4 %8, %18, off sc1\n\tglobal_load_dwordx4 %9, %19, off sc1\n\ts_waitcnt vmcnt(0)"
                 : "=&v"(a[0]), "=&v"(a[1]), "=&v"(a[2]), "=&v"(a[3]), "=&v"(a[4]), "=&v"(b[0]), "=&v"(b[1]), "=&v"(b[2]), "=&v"(b[3]), "=&v"(b[4])
                 : "v"(p.acc + e0), "v"(p.acc + SL + e0), "v"(p.acc + 2 * SL + e0), "v"(p.acc + 3 * SL + e0), "v"(p.acc + 4 * SL + e0),
                   "v"(p.acc + e1), "v"(p.acc + SL + e1), "v"(p.acc + 2 * SL + e1), "v"(p.acc + 3 * SL + e1), "v"(p.acc + 4 * SL + e1) : "memory");
    f32x4 sa = (a[0] + a[1]) + (a[2] + a[3]) + a[4], sb = (b[0] + b[1]) + (b[2] + b[3]) + b[4];
    bf16x4 bv = *reinterpret_cast<const bf16x4*>(p.b2 + (e0 & (DM - 1)));
    bf16x4 x0 = *reinterpret_cast<const bf16x4*>(p.x + e0), x1 = *reinterpret_cast<const bf16x4*>(p.x + e1);
    bf16x4 o0, o1;
#pragma unroll
    for (int r = 0; r < 4; r++) {
      o0[r] = (short)f2bf(bf2f((u16)x0[r]) + sa[r] + bf2f((u16)bv[r]));
      o1[r] = (short)f2bf(bf2f((u16)x1[r]) + sb[r] + bf2f((u16)bv[r]));
    }
    *reinterpret_cast<bf16x4*>(p.out + e0) = o0;
    *reinterpret_cast<bf16x4*>(p.out + e1) = o1;
  }
}

__device__ __forceinline__ void attn_task(char* smem, const MegaP& p, int s, int hg) {
  int tid = threadIdx.x;
  asm volatile("" : "+v"(tid));
  const int w = __builtin_amdgcn_readfirstlane(tid >> 6), lane = tid & 63, fr = lane & 15, g = lane >> 4;
  const int h = hg * 8 + w;
  char* sV = smem + w * 32 * KROW;
  const u16* tok = p.qkv + (size_t)s * SEG * NQKV + h * 128;
#pragma unroll
  for (int i = 0; i < 8; i++) {
    int c = i * 64 + lane, row = c >> 4, ch = c & 15;
    *reinterpret_cast<u32x4*>(sV + row * KROW + ch * 16) = *reinterpret_cast<const u32x4*>(tok + (size_t)row * NQKV + 8192 + ch * 8);
  }
  bf16x8 kt[2][4];
#pragma unroll
  for (int t = 0; t < 2; t++)
#pragma unroll
    for (int ks = 0; ks < 4; ks++) kt[t][ks] = *reinterpret_cast<const bf16x8*>(tok + (size_t)(16 * t + fr) * NQKV + 4096 + ks * 32 + g * 8);
  u32x4 k0, k1, k2, k3;
  u32x2 v0, v1, v2, v3, v4, v5, v6, v7;
  {
    const u16* eb = p.qkvm + ((size_t)s * NH + h) * 3072;
    const u16* qp = eb + (g >> 1) * 128 + (fr & 7) * 16 + (g & 1) * 8;
    const u16* vp = eb + 512 + fr * 8 + (g & 1) * 4;
    unsigned spins = 0;
    while (true) {
      asm volatile(
          "global_load_dwordx4 %0, %12, off offset:512 sc1\n\t"
          "global_load_dwordx4 %1, %12, off offset:2048 sc1\n\t"
          "global_load_dwordx4 %2, %12, off offset:3584 sc1\n\t"
          "global_load_dwordx4 %3, %13, off offset:1024 sc1\n\t"
          "global_load_dwordx2 %4, %14, off sc1\n\t"
          "global_load_dwordx2 %5, %14, off offset:256 sc1\n\t"
          "global_load_dwordx2 %6, %14, off offset:1536 sc1\n\t"
          "global_load_dwordx2 %7, %14, off offset:1792 sc1\n\t"
          "global_load_dwordx2 %8, %14, off offset:3072 sc1\n\t"
          "global_load_dwordx2 %9, %14, off offset:3328 sc1\n\t"
          "global_load_dwordx2 %10, %15, off offset:512 sc1\n\t"
          "global_load_dwordx2 %11, %15, off offset:768 sc1\n\t"
          "s_waitcnt vmcnt(0)"
          : "=&v"(k0), "=&v"(k1), "=&v"(k2), "=&v"(k3), "=&v"(v0), "=&v"(v1), "=&v"(v2), "=&v"(v3), "=&v"(v4), "=&v"(v5), "=&v"(v6), "=&v"(v7)
          : "v"(qp), "v"(qp + 2048), "v"(vp), "v"(vp + 2048) : "memory");
      bool bad = poisoned(k0) | poisoned(k1) | poisoned(k2) | poisoned(k3);
      bad |= (v0[0] == 0xFFFFFFFFu) | (v0[1] == 0xFFFFFFFFu) | (v1[0] == 0xFFFFFFFFu) | (v1[1] == 0xFFFFFFFFu) |
             (v2[0] == 0xFFFFFFFFu) | (v2[1] == 0xFFFFFFFFu) | (v3[0] == 0xFFFFFFFFu) | (v3[1] == 0xFFFFFFFFu) |
             (v4[0] == 0xFFFFFFFFu) | (v4[1] == 0xFFFFFFFFu) | (v5[0] == 0xFFFFFFFFu) | (v5[1] == 0xFFFFFFFFu) |
             (v6[0] == 0xFFFFFFFFu) | (v6[1] == 0xFFFFFFFFu) | (v7[0] == 0xFFFFFFFFu) | (v7[1] == 0xFFFFFFFFu);
      if (!__any(bad)) break;
      __builtin_amdgcn_s_sleep(32);
      if (++spins > 4000000u) { if (lane == 0) __hip_atomic_store((gu32*)p.ctl, 5u, __ATOMIC_RELAXED, __HIP_MEMORY_SCOPE_AGENT); break; }
    }
  }
  bf16x8 km[4] = {__builtin_bit_cast(bf16x8, k0), __builtin_bit_cast(bf16x8, k1), __builtin_bit_cast(bf16x8, k2), __builtin_bit_cast(bf16x8, k3)};
  u32x2 vv[8] = {v0, v1, v2, v3, v4, v5, v6, v7};
  __builtin_amdgcn_s_waitcnt(0);
  __builtin_amdgcn_wave_barrier();
#pragma unroll
  for (int qt = 0; qt < 2; qt++) {
    const int qi = qt * 16 + fr;
    f32x4 st[3];
#pragma unroll
    for (int t = 0; t < 3; t++) st[t] = f32x4{0.f, 0.f, 0.f, 0.f};
#pragma unroll
    for (int ks = 0; ks < 4; ks++) {
      bf16x8 qf = *reinterpret_cast<const bf16x8*>(tok + (size_t)qi * NQKV + ks * 32 + g * 8);
      st[0] = __builtin_amdgcn_mfma_f32_16x16x32_bf16(kt[0][ks], qf, st[0], 0, 0, 0);
      st[1] = __builtin_amdgcn_mfma_f32_16x16x32_bf16(kt[1][ks], qf, st[1], 0, 0, 0);
      st[2] = __builtin_amdgcn_mfma_f32_16x16x32_bf16(km[ks], qf, st[2], 0, 0, 0);
    }
    float mx = -3.0e38f;
#pragma unroll
    for (int t = 0; t < 3; t++)
#pragma unroll
      for (int r = 0; r < 4; r++) {
        bool masked = t < 2 ? (16 * t + 4 * g + r > qi) : (g >= 2);
        float v = masked ? -3.0e38f : st[t][r];
        st[t][r] = v;
        mx = fmaxf(mx, v);
      }
    mx = fmaxf(mx, xor16(mx));
    mx = fmaxf(mx, xor32(mx));
    const float c2 = SCALE * 1.4426950408889634f;
    float mc = mx * c2, sum = 0.f;
#pragma unroll
    for (int t = 0; t < 3; t++)
#pragma unroll
      for (int r = 0; r < 4; r++) {
        float e = __builtin_amdgcn_exp2f(__builtin_fmaf(st[t][r], c2, -mc));
        st[t][r] = e;
        sum += e;
      }
    sum += xor16(sum);
    sum += xor32(sum);
    float inv = __builtin_amdgcn_rcpf(sum);
    bf16x8 pf[2];
#pragma unroll
    for (int r = 0; r < 4; r++) {
      pf[0][r] = (short)f2bf(st[0][r] * inv);
      pf[0][4 + r] = (short)f2bf(st[1][r] * inv);
      pf[1][r] = (short)f2bf(st[2][r] * inv);
      pf[1][4 + r] = 0;
    }
    int q = fr >> 2, pp = fr & 3;
    u16* op = p.O + (size_t)(s * SEG + qi) * 4096 + h * 128;
#pragma unroll
    for (int et = 0; et < 8; et++) {
      bf16x4 lo = __builtin_amdgcn_ds_read_tr16_b64_v4i16((lds_b4*)(sV + (4 * g + q) * KROW + et * 32 + pp * 8));
      bf16x4 hi = __builtin_amdgcn_ds_read_tr16_b64_v4i16((lds_b4*)(sV + (16 + 4 * g + q) * KROW + et * 32 + pp * 8));
      bf16x8 vf = bf16x8{lo[0], lo[1], lo[2], lo[3], hi[0], hi[1], hi[2], hi[3]};
      f32x4 o = {0.f, 0.f, 0.f, 0.f};
      o = __builtin_amdgcn_mfma_f32_16x16x32_bf16(vf, pf[0], o, 0, 0, 0);
      u32x4 vx = {vv[et][0], vv[et][1], 0u, 0u};
      o = __builtin_amdgcn_mfma_f32_16x16x32_bf16(__builtin_bit_cast(bf16x8, vx), pf[1], o, 0, 0, 0);
      bf16x4 ov;
#pragma unroll
      for (int r = 0; r < 4; r++) ov[r] = (short)f2bf(o[r]);
      st8_sc1(op + et * 16 + g * 4, ov);
    }
  }
}

constexpr int T_K0 = 128, T_KV = T_K0 + 448, T_Q = T_KV + 256, T_H = T_Q + 224, T_B = T_H + 64, T_END = T_B + 16 * 48;
constexpr int MEGA_LDS = 152 * 1024;

__global__ __launch_bounds__(768, 3) void mega_kernel(MegaP p) {
  extern __shared__ __attribute__((aligned(16))) char smem[];
  if (blockIdx.x < 128) {
    rec_role(smem, p.Wq, p.Wk, p.Wv, p.Wo, p.qkv, p.memx, p.qkvm, p.part, p.ctl);
    return;
  }
  if (threadIdx.x >= 512) return;
  volatile unsigned* bc = reinterpret_cast<volatile unsigned*>(smem + 150 * 1024 + 16);
  while (true) {
    __syncthreads();
    if (threadIdx.x == 0) *bc = __hip_atomic_fetch_add((gu32*)(p.ctl + C_TICKET), 1u, __ATOMIC_RELAXED, __HIP_MEMORY_SCOPE_AGENT);
    __syncthreads();
    int t = (int)*bc;
    if (t >= T_END) break;
    if (t < T_K0) {
      int half = t >> 6, n2 = t & 63;
      gemm_tile<128>(1, smem, p.xn, DM, n2 < 32 ? p.Wk : p.Wv, 4096, half * 128, (n2 & 31) * 128, 0, DM / BK, p.qkv, NQKV, 4096 + n2 * 128, nullptr);
      signal_count(p.ctl + C_KV, smem);
    } else if (t < T_B) {
      int epi, m, nt, k0, nkt, lda, ldb, ldo; const u16* A; const u16* B; void* out; unsigned* sig;
      int ocol_add = 0;
      if (t < T_KV) {
        m = 1 + ((t - T_K0) >> 6); int n2 = (t - T_K0) & 63; nt = n2 & 31; epi = 1; A = p.xn; lda = DM; B = n2 < 32 ? p.Wk : p.Wv; ldb = 4096; k0 = 0; nkt = DM / BK; out = p.qkv; ldo = NQKV; sig = p.ctl + C_KV + m * 32;
        ocol_add = n2 < 32 ? 4096 : 8192;
      } else if (t < T_Q) {
        int u = t - T_KV; m = u >> 5; nt = u & 31; epi = 1; A = p.xn; lda = DM; B = p.Wq; ldb = 4096; k0 = 0; nkt = DM / BK; out = p.qkv; ldo = NQKV; sig = p.ctl + C_Q + m * 32;
      } else if (t < T_H) {
        int u = t - T_Q; m = u / 28; nt = u % 28; epi = 2; A = p.xn; lda = DM; B = p.w1; ldb = FF; k0 = 0; nkt = DM / BK; out = p.H; ldo = FF; sig = p.ctl + C_H + m * 32;
      } else {
        int u = t - T_H; m = u >> 3; nt = u & 7; epi = 3; A = p.H; lda = FF; B = p.w2; ldb = DM; k0 = 0; nkt = 56; out = p.acc + 4 * (size_t)S_ * DM; ldo = DM; sig = p.ctl + C_F + (2 * m * 8 + nt) * 16;
        wait_count(p.ctl + C_H + m * 32, 28, p.ctl);
      }
      gemm_tile<256>(epi, smem, A, lda, B, ldb, m * 256, nt * 128, k0, nkt, out, ldo, ocol_add + nt * 128, p.b1);
      unsigned prev = signal_count(sig, smem);
      if (epi == 3) {
        if (prev == 4) final_tile(p, 2 * m, nt);
        prev = signal_count(sig + 8 * 16, smem);
        if (prev == 4) final_tile(p, 2 * m + 1, nt);
      }
    } else {
      int u = t - T_B, mt = u / 48, v = u % 48;
      if (v < 16) {
        int s = mt * 4 + (v >> 2), hg = v & 3;
        wait_count(p.ctl + C_KV + (mt >> 1) * 32, mt < 2 ? 128 : 64, p.ctl);
        wait_count(p.ctl + C_Q + (mt >> 1) * 32, 32, p.ctl);
        attn_task(smem, p, s, hg);
        signal_count(p.ctl + C_A + mt * 32, smem);
      } else {
        int d = v - 16, nt = d >> 2, ks = d & 3;
        wait_count(p.ctl + C_A + mt * 32, 16, p.ctl);
        gemm_tile<128>(3, smem, p.O, 4096, p.Wo, DM, mt * 128, nt * 128, ks * 1024, 16, p.acc + (size_t)ks * S_ * DM, DM, nt * 128, nullptr);
        unsigned prev = signal_count(p.ctl + C_F + (mt * 8 + nt) * 16, smem);
        if (prev == 4) final_tile(p, mt, nt);
      }
    }
  }
}

extern "C" void kernel_launch(void* const* d_in, const int* in_sizes, int n_in, void* d_out, int out_size,
                              void* d_ws, size_t ws_size, hipStream_t stream) {
  char* ws = (char*)d_ws;
  MegaP p;
  p.x = (const u16*)d_in[0];
  const u16* mem0 = (const u16*)d_in[1];
  p.Wq = (const u16*)d_in[2]; p.Wk = (const u16*)d_in[3]; p.Wv = (const u16*)d_in[4]; p.Wo = (const u16*)d_in[5];
  p.w1 = (const u16*)d_in[8]; p.b1 = (const u16*)d_in[9]; p.w2 = (const u16*)d_in[10]; p.b2 = (const u16*)d_in[11];
  p.xn = (u16*)(ws + WS_XN); p.qkv = (u16*)(ws + WS_QKV); p.H = (u16*)(ws + WS_H); p.O = (u16*)(ws + WS_O);
  p.memx = (u16*)(ws + WS_MEM); p.qkvm = (u16*)(ws + WS_QKVM); p.part = (float*)(ws + WS_PART);
  p.acc = (float*)(ws + WS_ACC); p.ctl = (unsigned*)(ws + WS_TMO); p.out = (u16*)d_out;

  (void)hipFuncSetAttribute((const void*)mega_kernel, hipFuncAttributeMaxDynamicSharedMemorySize, MEGA_LDS);

  (void)hipMemsetAsync(ws + WS_TMO, 0, WS_ZERO_BYTES, stream);
  ln_kernel<<<S_ / 4, 256, 0, stream>>>(p.x, mem0, p.xn, p.memx, p.qkvm, p.part, p.acc);
  mega_kernel<<<256, 768, MEGA_LDS, stream>>>(p);
}
```
